```python
import torch

def _softmax(x, axis=-1):
    return torch.softmax(x, dim=axis)
def _gelu(x):
    return torch.nn.functional.gelu(x, approximate="tanh")
def _silu(x):
    return torch.nn.functional.silu(x)
def _sigmoid(x):
    return torch.sigmoid(x)
def _layernorm(x, axis=-1, eps=1e-5):
    m = x.mean(dim=axis, keepdim=True)
    v = x.var(dim=axis, keepdim=True, unbiased=False)
    return (x - m) / torch.sqrt(v + eps)
def _scatter_add(tgt, idx, src, axis=0):
    return tgt.clone().index_add_(axis, idx, src)
def _segment_sum(src, seg, n):
    out = torch.zeros((n,) + src.shape[1:], dtype=src.dtype)
    return out.index_add_(0, seg, src)
def _pad(x, pw):
    flat = tuple(v for pair in reversed(pw) for v in pair)
    return torch.nn.functional.pad(x, flat)

kernel_name = 'sequential_preLN__linear_attn_block__swiglu_ffn__prompt'

def setup_inputs():

    _w = (lambda a: (lambda t: t.to(torch.bfloat16) if t.is_floating_point() else t)(torch.as_tensor(a)))
    def _A_setup():
        B, S, D = 1, 2048, 640
        return {
            "x": _w((lambda *s: torch.randn(*s, dtype=torch.bfloat16) * 0.05)(B, S, D)),
            "wqkv": _w((lambda *s: torch.randn(*s, dtype=torch.bfloat16) * 0.1)(D, 3 * D)),
            "wo": _w((lambda *s: torch.randn(*s, dtype=torch.bfloat16) * 0.05)(D, D)),
        }
    _A_in = _A_setup()

    def _B_setup():
        B, S, D, F = 1, 2048, 640, 1728
        return {
            "x": _w((lambda *s: torch.randn(*s, dtype=torch.bfloat16) * 0.05)(B, S, D)),
            "w_gate": _w((lambda *s: torch.randn(*s, dtype=torch.bfloat16) * 0.05)(D, F)),
            "w_up": _w((lambda *s: torch.randn(*s, dtype=torch.bfloat16) * 0.05)(D, F)),
            "w_down": _w((lambda *s: torch.randn(*s, dtype=torch.bfloat16) * 0.05)(F, D)),
        }
    _B_in = _B_setup()

    return {
        "A_x": _A_in["x"],
        "A_wqkv": _A_in["wqkv"],
        "A_wo": _A_in["wo"],
        "B_x": _B_in["x"],
        "B_w_gate": _B_in["w_gate"],
        "B_w_up": _B_in["w_up"],
        "B_w_down": _B_in["w_down"],
    }

def reference(A_x, A_wqkv, A_wo, B_x, B_w_gate, B_w_up, B_w_down):
    def _A_ref(x, wqkv, wo):
        B, S, D = x.shape
        H = 32; Dh = D // H
        qkv = (lambda x, *d: x.permute(*d))((x @ wqkv).reshape(B, S, 3, H, Dh), 2, 0, 3, 1, 4)
        q, k, v = qkv[0], qkv[1], qkv[2]
        phi = lambda t: torch.where(t > 0, t + 1, torch.exp(t)) + 1e-6
        pq, pk = phi(q), phi(k)
        kv = torch.einsum("bhsd,bhse->bhde", pk, v)
        z = torch.einsum("bhsd->bhd", pk)
        num = torch.einsum("bhqd,bhde->bhqe", pq, kv)
        den = torch.einsum("bhqd,bhd->bhq", pq, z)[..., None] + 1e-6
        o = num / den
        return (lambda x, *d: x.permute(*d))(o, 0, 2, 1, 3).reshape(B, S, D) @ wo

    def _B_ref(x, w_gate, w_up, w_down):
        g = _silu(x @ w_gate)
        u = x @ w_up
        return (g * u) @ w_down

    x = A_x
    h = x + _A_ref(_layernorm(x, axis=-1), A_wqkv, A_wo)
    h2 = h + _B_ref(_layernorm(h, axis=-1), B_w_gate, B_w_up, B_w_down)
    return h2
```

```cpp
#include <hip/hip_runtime.h>
#include <hip/hip_bf16.h>
typedef unsigned short u16;
__device__ __forceinline__ float bf2f(u16 b){ return __uint_as_float(((unsigned)b)<<16); }
__device__ __forceinline__ u16 f2bf(float f){ unsigned u=__float_as_uint(f); u += 0x7fffu + ((u>>16)&1u); return (u16)(u>>16); }
constexpr int S=2048, D=640, F=1728, H=32, Dh=20;

__global__ void ln_k(const float* xin_f, const u16* xin_b, float* out){
  int r=blockIdx.x; int l=threadIdx.x; float v[10]; float s=0;
  for(int i=0;i<10;i++){ v[i]= xin_f? xin_f[r*D+l+64*i] : bf2f(xin_b[r*D+l+64*i]); s+=v[i]; }
  for(int o=32;o;o>>=1) s+=__shfl_xor(s,o);
  float m=s/D; float q=0; for(int i=0;i<10;i++){ float d=v[i]-m; q+=d*d; }
  for(int o=32;o;o>>=1) q+=__shfl_xor(q,o);
  float rs=rsqrtf(q/D+1e-5f);
  for(int i=0;i<10;i++) out[r*D+l+64*i]=(v[i]-m)*rs;
}
__global__ void gemm_k(const float* A, const u16* W, float* C, int M,int N,int K){
  __shared__ float As[16][17]; __shared__ float Ws[16][17];
  int tx=threadIdx.x&15, ty=threadIdx.x>>4; int row=blockIdx.y*16+ty, col=blockIdx.x*16+tx; float acc=0;
  for(int k0=0;k0<K;k0+=16){ As[ty][tx]=A[row*K+k0+tx]; Ws[ty][tx]=bf2f(W[(k0+ty)*N+col]); __syncthreads();
    for(int k=0;k<16;k++) acc+=As[ty][k]*Ws[k][tx]; __syncthreads(); }
  C[row*N+col]=acc;
}
__device__ __forceinline__ float phi(float t){ return (t>0? t+1.f : __expf(t)) + 1e-6f; }
__global__ void kv_k(const float* qkv, float* kv){
  int h=blockIdx.x; int t=threadIdx.x; if(t>=420) return; int d=t/21, e=t%21; float acc=0;
  for(int s=0;s<S;s++){ float pk=phi(qkv[s*1920+640+h*20+d]); float v= e<20? qkv[s*1920+1280+h*20+e]:1.f; acc+=pk*v; }
  kv[h*420+t]=acc;
}
__global__ void o_k(const float* qkv, const float* kv, float* o){
  int s=blockIdx.x; int c=threadIdx.x; int h=c/20,e=c%20; float num=0,den=0;
  for(int d=0;d<20;d++){ float pq=phi(qkv[s*1920+h*20+d]); num+=pq*kv[h*420+d*21+e]; den+=pq*kv[h*420+d*21+20]; }
  o[s*D+c]=num/(den+1e-6f);
}
__global__ void addx_k(const u16* x, float* h){ int i=blockIdx.x*256+threadIdx.x; h[i]+=bf2f(x[i]); }
__global__ void act_k(float* g, const float* u){ int i=blockIdx.x*256+threadIdx.x; float a=g[i]; g[i]=a/(1.f+__expf(-a))*u[i]; }
__global__ void out_k(const float* h, const float* y, u16* out){ int i=blockIdx.x*256+threadIdx.x; out[i]=f2bf(h[i]+y[i]); }

extern "C" void kernel_launch(void* const* d_in, const int* in_sizes, int n_in, void* d_out, int out_size, void* d_ws, size_t ws_size, hipStream_t stream) {
  const u16* x=(const u16*)d_in[0]; const u16* wqkv=(const u16*)d_in[1]; const u16* wo=(const u16*)d_in[2];
  const u16* wg=(const u16*)d_in[4]; const u16* wu=(const u16*)d_in[5]; const u16* wd=(const u16*)d_in[6];
  float* ws=(float*)d_ws; float* xn=ws; float* qkv=xn+S*D; float* kv=qkv+S*1920; float* o=kv+16384; float* h=o+S*D; float* g=h+S*D; float* u=g+S*F; float* y=u+S*F;
  ln_k<<<S,64,0,stream>>>(nullptr,x,xn);
  gemm_k<<<dim3(1920/16,S/16),256,0,stream>>>(xn,wqkv,qkv,S,1920,D);
  kv_k<<<H,448,0,stream>>>(qkv,kv);
  o_k<<<S,640,0,stream>>>(qkv,kv,o);
  gemm_k<<<dim3(D/16,S/16),256,0,stream>>>(o,wo,h,S,D,D);
  addx_k<<<S*D/256,256,0,stream>>>(x,h);
  ln_k<<<S,64,0,stream>>>(h,nullptr,xn);
  gemm_k<<<dim3(F/16,S/16),256,0,stream>>>(xn,wg,g,S,F,D);
  gemm_k<<<dim3(F/16,S/16),256,0,stream>>>(xn,wu,u,S,F,D);
  act_k<<<S*F/256,256,0,stream>>>(g,u);
  gemm_k<<<dim3(D/16,S/16),256,0,stream>>>(g,wd,y,S,D,F);
  out_k<<<S*D/256,256,0,stream>>>(h,y,(u16*)d_out);
}
```

```cpp
#include <hip/hip_runtime.h>
#include <utility>

#define DI __device__ __forceinline__
#define LAS __attribute__((address_space(3)))
#define GAS __attribute__((address_space(1)))
typedef unsigned u32;
typedef u32 v4u __attribute__((ext_vector_type(4)));
typedef u32 v2u __attribute__((ext_vector_type(2)));
typedef float f32x2 __attribute__((ext_vector_type(2)));
typedef float f32x4 __attribute__((ext_vector_type(4)));
typedef float f32x16 __attribute__((ext_vector_type(16)));
typedef __bf16 bf16x2 __attribute__((ext_vector_type(2)));
typedef __amdgpu_buffer_rsrc_t rsrc_t;
#define ACLOB "a0","a1","a2","a3","a4","a5","a6","a7","a8","a9","a10","a11","a12","a13","a14","a15","a16","a17","a18","a19","a20","a21","a22","a23","a24","a25","a26","a27","a28","a29","a30","a31","a32","a33","a34","a35","a36","a37","a38","a39","a40","a41","a42","a43","a44","a45","a46","a47","a48","a49","a50","a51","a52","a53","a54","a55","a56","a57","a58","a59","a60","a61","a62","a63","a64","a65","a66","a67","a68","a69","a70","a71","a72","a73","a74","a75","a76","a77","a78","a79","a80","a81","a82","a83","a84","a85","a86","a87","a88","a89","a90","a91","a92","a93","a94","a95","a96","a97","a98","a99","a100","a101","a102","a103","a104","a105","a106","a107","a108","a109","a110","a111","a112","a113","a114","a115","a116","a117","a118","a119","a120","a121","a122","a123","a124","a125","a126","a127","a128","a129","a130","a131","a132","a133","a134","a135","a136","a137","a138","a139","a140","a141","a142","a143","a144","a145","a146","a147","a148","a149","a150","a151","a152","a153","a154","a155","a156","a157","a158","a159","a160","a161","a162","a163","a164","a165","a166","a167","a168","a169","a170","a171","a172","a173","a174","a175","a176","a177","a178","a179","a180","a181","a182","a183","a184","a185","a186","a187","a188","a189","a190","a191","a192","a193","a194","a195","a196","a197","a198","a199","a200","a201","a202","a203","a204","a205","a206","a207","a208","a209","a210","a211","a212","a213","a214","a215","a216","a217","a218","a219","a220","a221","a222","a223","a224","a225","a226","a227","a228","a229","a230","a231","a232","a233","a234","a235","a236","a237","a238","a239","a240","a241","a242","a243","a244","a245","a246","a247","a248","a249","a250","a251","a252","a253","a254","a255"

constexpr u32 WS_EPOCH = 0;
constexpr u32 WS_FLG = 4096;
constexpr u32 WS_WA = WS_FLG + 6 * 32768;
constexpr u32 WA_G = 153600;
constexpr u32 WS_CSA = WS_WA + 16 * WA_G + 4096;
constexpr u32 WS_WB = WS_CSA + 16 * 16 * 512;
constexpr u32 WS_WC = WS_WB + 8 * 102400;
constexpr u32 WC_G = 286720;
constexpr u32 WS_CSC = WS_WC + 16 * WC_G;
constexpr u32 WS_WD = WS_CSC + 16 * 16 * 1024;
constexpr u32 WS_KVP = WS_WD + 8 * 286720;
constexpr u32 WS_O = WS_KVP + 256 * 3456;
constexpr u32 WS_H = WS_O + 128 * 20480;
constexpr u32 WS_HST = WS_H + 64 * 40960;
constexpr u32 WS_ACT = WS_HST + 128 * 1024;
constexpr u32 WS_END = WS_ACT + 128 * 57344;
#define ST(k)
constexpr int LDS_BYTES = 163840;
constexpr u32 CODE_BYTES = 40960;
constexpr u32 K1 = 1024;

template <class F, int... Is> DI void sfor_(F&& f, std::integer_sequence<int, Is...>) { (f(std::integral_constant<int, Is>{}), ...); }
template <int N, class F> DI void sfor(F&& f) { sfor_(f, std::make_integer_sequence<int, N>{}); }
#define IC(x) decltype(x)::value

#define GAP() __builtin_amdgcn_sched_barrier(0)
#define BAR() asm volatile("s_waitcnt lgkmcnt(0)\n\ts_barrier" ::: "memory", ACLOB)
template <int N> DI void waitv() { asm volatile("s_waitcnt vmcnt(%c0)" ::"i"(N > 63 ? 63 : N) : "memory"); }
DI void waitl() { asm volatile("s_waitcnt lgkmcnt(0)" ::: "memory"); }
DI void mfma_done() { GAP(); asm volatile("s_nop 7\n\ts_nop 7" ::: "memory"); GAP(); }
DI void valu_done() { GAP(); asm volatile("s_nop 1" ::: "memory"); GAP(); }

template <int A, int IMM> DI void ld_a(u32 voff, rsrc_t rs, u32 soff) {
  asm volatile("buffer_load_dwordx4 a[%c3:%c4], %0, %1, %2 offen offset:%c5 sc1" ::"v"(voff), "s"(rs), "s"(soff), "i"(A), "i"(A + 3), "i"(IMM) : "memory");
}
template <int N, bool SC1> DI void dma(u32 voff, rsrc_t rs, u32 soff, u32 ldsa) {
#define DMA1(o, s) "\n\tbuffer_load_dwordx4 %0, %1, %2 offen offset:" #o s " lds"
#define DMAH "s_mov_b32 m0, %3\n\ts_nop 0"
#define DMAN(s) \
  if constexpr (N == 1) asm volatile(DMAH DMA1(0, s)::"v"(voff), "s"(rs), "s"(soff), "s"(ldsa) : "memory"); \
  if constexpr (N == 2) asm volatile(DMAH DMA1(0, s) DMA1(1024, s)::"v"(voff), "s"(rs), "s"(soff), "s"(ldsa) : "memory"); \
  if constexpr (N == 3) asm volatile(DMAH DMA1(0, s) DMA1(1024, s) DMA1(2048, s)::"v"(voff), "s"(rs), "s"(soff), "s"(ldsa) : "memory"); \
  if constexpr (N == 4) asm volatile(DMAH DMA1(0, s) DMA1(1024, s) DMA1(2048, s) DMA1(3072, s)::"v"(voff), "s"(rs), "s"(soff), "s"(ldsa) : "memory");
  if constexpr (SC1) { DMAN(" sc1") } else { DMAN("") }
}
template <int N> DI void dma_lin(u32 voff, rsrc_t rs, u32 soff, u32 ldsa) {
  sfor<(N + 3) / 4>([&](auto i) { constexpr int r = (N - 4 * IC(i)) < 4 ? (N - 4 * IC(i)) : 4; dma<r, true>(voff, rs, soff + 4096u * IC(i), ldsa + 4096u * IC(i)); });
}
template <int I> DI void dma_p(u32 voff, rsrc_t rs, u32 soff, u32 ldsa) {
  if constexpr ((I & 3) == 0) dma<1, true>(voff, rs, soff + 1024u * I, ldsa + 1024u * I);
  else asm volatile("buffer_load_dwordx4 %0, %1, %2 offen offset:%c3 sc1 lds" ::"v"(voff), "s"(rs), "s"(soff + 1024u * (I & ~3)), "i"(1024 * (I & 3)) : "memory");
}
template <int A, int IMM> DI void ds_a(u32 addr) {
  asm volatile("ds_read_b128 a[%c1:%c2], %0 offset:%c3" ::"v"(addr), "i"(A), "i"(A + 3), "i"(IMM) : "memory");
}
DI v4u tr2(u32 a0, u32 a1) {
  v2u lo, hi;
  asm volatile("ds_read_b64_tr_b16 %0, %2\n\tds_read_b64_tr_b16 %1, %3\n\ts_waitcnt lgkmcnt(0)" : "=&v"(lo), "=&v"(hi) : "v"(a0), "v"(a1) : "memory");
  return v4u{lo.x, lo.y, hi.x, hi.y};
}
#define M16 "v_mfma_f32_16x16x32_bf16 "
#define M32 "v_mfma_f32_32x32x16_bf16 "
template <int A, bool Z> DI void m16_av(f32x4& c, const v4u& b) {
  if constexpr (Z) asm volatile(M16 "%0, a[%c2:%c3], %1, 0" : "=&v"(c) : "v"(b), "i"(A), "i"(A + 3));
  else asm volatile(M16 "%0, a[%c2:%c3], %1, %0" : "+v"(c) : "v"(b), "i"(A), "i"(A + 3));
}
template <int B, bool Z> DI void m16_va(f32x4& c, const v4u& a) {
  if constexpr (Z) asm volatile(M16 "%0, %1, a[%c2:%c3], 0" : "=&v"(c) : "v"(a), "i"(B), "i"(B + 3));
  else asm volatile(M16 "%0, %1, a[%c2:%c3], %0" : "+v"(c) : "v"(a), "i"(B), "i"(B + 3));
}
template <int A, int B, bool Z> DI void m16_aa(f32x4& c) {
  if constexpr (Z) asm volatile(M16 "%0, a[%c1:%c2], a[%c3:%c4], 0" : "=&v"(c) : "i"(A), "i"(A + 3), "i"(B), "i"(B + 3));
  else asm volatile(M16 "%0, a[%c1:%c2], a[%c3:%c4], %0" : "+v"(c) : "i"(A), "i"(A + 3), "i"(B), "i"(B + 3));
}
template <bool Z> DI void m16_vv(f32x4& c, const v4u& a, const v4u& b) {
  if constexpr (Z) asm volatile("s_nop 1\n\t" M16 "%0, %1, %2, 0" : "=&v"(c) : "v"(a), "v"(b));
  else asm volatile("s_nop 1\n\t" M16 "%0, %1, %2, %0" : "+v"(c) : "v"(a), "v"(b));
}
template <int B, bool Z> DI void m32_va(f32x16& c, const v4u& a) {
  if constexpr (Z) asm volatile(M32 "%0, %1, a[%c2:%c3], 0" : "=&v"(c) : "v"(a), "i"(B), "i"(B + 3));
  else asm volatile(M32 "%0, %1, a[%c2:%c3], %0" : "+v"(c) : "v"(a), "i"(B), "i"(B + 3));
}
DI u32 cvtpk(float lo, float hi) { f32x2 v = {lo, hi}; return __builtin_bit_cast(u32, __builtin_convertvector(v, bf16x2)); }
DI float bflo(u32 u) { return __builtin_bit_cast(float, u << 16); }
DI float bfhi(u32 u) { return __builtin_bit_cast(float, u & 0xffff0000u); }
DI float sum8(v4u v) { return ((bflo(v.x) + bfhi(v.x)) + (bflo(v.y) + bfhi(v.y))) + ((bflo(v.z) + bfhi(v.z)) + (bflo(v.w) + bfhi(v.w))); }
DI float phi(float t) { return (fmaxf(t, 0.f) + __builtin_amdgcn_exp2f(fminf(t, 0.f) * 1.4426950408889634f)) + 1e-6f; }
template <class T> DI T lds_ld(u32 a) { return *(const LAS T*)(size_t)a; }
template <class T> DI void lds_st(u32 a, T v) { *(LAS T*)(size_t)a = v; }
DI void st16(v4u v, rsrc_t rs, u32 voff, u32 soff) {
  asm volatile("buffer_store_dwordx4 %0, %1, %2, %3 offen sc1\n\ts_nop 1" ::"v"(v), "v"(voff), "s"(rs), "s"(soff) : "memory");
}
DI void st8(v2u v, rsrc_t rs, u32 voff, u32 soff) { __builtin_amdgcn_raw_buffer_store_b64(v, rs, voff, soff, 16); }
DI void st4(u32 v, rsrc_t rs, u32 voff, u32 soff) { __builtin_amdgcn_raw_buffer_store_b32(v, rs, voff, soff, 16); }
DI rsrc_t mkrs(const void* p, u32 bytes) { return __builtin_amdgcn_make_buffer_rsrc((void*)p, 0, bytes, 0x00020000); }
constexpr u32 OOB = 0x7ff00000u;

DI void poll(rsrc_t rs, u32 voff, u32 tag) {
#pragma unroll 1
  for (u32 spins = 0; spins < (1u << 16); ++spins) {
    const u32 v = __builtin_amdgcn_raw_buffer_load_b32(rs, voff, 0, 16);
    if (__all(v == tag)) break;
    asm volatile("" ::: "memory");
  }
  asm volatile("" ::: "memory");
}

constexpr int C_UP = 8, C_WD0 = 8;
struct CPlan { int kind[120], idx[120], before[121], hpos[40], rlast[10]; };
constexpr CPlan make_cplan() {
  CPlan P{};
  int nh = C_UP, nr = 0, nf = C_WD0, n = 2 + C_UP;
  for (int i = 0; i < C_UP; ++i) P.hpos[i] = 2 + i;
  for (int s = 0; s < 120; ++s) {
    const int ks = s / 3, pos = s % 3, avail = 7 * ((ks + 1) / 4);
    P.before[s] = n;
    int k = 0;
    if (nh < 40 && pos < 2) k = 1;
    else if (nr < avail && nr < 63) k = 2;
    else if (nf < 24) k = 3;
    else if (nh < 40) k = 1;
    P.kind[s] = k;
    if (k == 1) { P.idx[s] = nh; P.hpos[nh++] = n++; }
    if (k == 2) { P.idx[s] = nr; if (nr % 7 == 6) P.rlast[nr / 7] = n; ++nr; ++n; }
    if (k == 3) { P.idx[s] = nf++; ++n; }
  }
  P.before[120] = n;
  P.rlast[9] = (nr == 63 && nf == 24 && nh == 40) ? 1 : -1;
  return P;
}
constexpr CPlan CP = make_cplan();
static_assert(CP.rlast[9] == 1, "plan incomplete");

__global__ __launch_bounds__(256, 1) void fused_block(const void* __restrict__ x_, const void* __restrict__ wqkv_, const void* __restrict__ wo_,
                                                      const void* __restrict__ wg_, const void* __restrict__ wu_, const void* __restrict__ wd_,
                                                      void* __restrict__ out_, void* ws_) {
  extern __shared__ __attribute__((aligned(16))) char lds_[];
  const u32 L0 = (u32)(size_t)(LAS char*)lds_;
  ST(0);
  const u32 b = blockIdx.x, xcd = b & 7, r_ = b >> 3;
  const u32 p = (xcd >> 1) * 4 + (r_ >> 3), ch = xcd & 1, cc = r_ & 7, g = 8 * ch + cc, n80 = 4 * ch + (cc & 3), half = cc >> 2;
  const u32 lane = threadIdx.x & 63, w = __builtin_amdgcn_readfirstlane(threadIdx.x >> 6);
  const u32 c = lane & 15, gm = lane >> 4, l16 = lane * 16;
  const rsrc_t rx = mkrs(x_, 2048 * 640 * 2), rws = mkrs(ws_, WS_END);
  u32 epoch;
  asm volatile("s_load_dword %0, %1, 0x0" : "=s"(epoch) : "s"(ws_) : "memory");
  asm volatile("s_nop 4" ::: "memory");
  const u32 myflag = WS_FLG + (p * 16 + g) * 128;
  const u32 xl = L0 + w * 40 * K1;

  if (w == 0) {
    const rsrc_t rw = mkrs(wqkv_, 640 * 1920 * 2);
    const u32 l = lane < 60 ? lane : 59, rr = l / 15, cq = l % 15;
    const u32 vo = rr * 3840 + (cq / 5) * 1280 + (cq % 5) * 16;
    sfor<10>([&](auto i) { dma<1, false>(vo, rw, (40 * p + 4 * IC(i)) * 3840 + g * 80, xl + (30 + IC(i)) * K1); });
  } else if (w == 3) {
    unsigned long long pc;
    asm volatile("s_getpc_b64 %0" : "=s"(pc));
    const rsrc_t rc = mkrs((const void*)(pc & ~255ull), CODE_BYTES);
    asm volatile("buffer_load_dwordx4 a[248:251], %0, %1, %2 offen\n\tbuffer_load_dwordx4 a[252:255], %0, %1, %2 offen offset:1024" ::"v"(l16), "s"(rc), "s"(r_ * 2048) : "memory");
  }
  ST(24);
  const u32 xvo = (lane >> 3) * 1280 + (((lane & 7) ^ ((lane >> 3) & 6)) * 16), xso = (128 * p + 32 * w) * 1280;
  auto xpiece = [&](auto i) { dma<1, false>(xvo, rx, xso + (IC(i) & 3) * 10240 + (IC(i) >> 2) * 128, xl + IC(i) * K1); };
  sfor<24>(xpiece);
  ST(25);
  u32 lb0 = L0 + l16, lb1 = lb0 + 61440u, lb2 = lb0 + 122880u;
  asm volatile("" : "+v"(lb0), "+v"(lb1), "+v"(lb2));
  auto LA = [&](u32 off) -> u32 { return (off < 61440u ? lb0 : off < 122880u ? lb1 : lb2) + off % 61440u; };
  const u32 q4 = c >> 2, pp = c & 3;
  if (w == 0) {
    const u32 col0 = gm == 0 ? 40 + 4 * pp : gm == 1 ? 60 + 4 * pp : gm == 2 ? 56 + 20 * pp : 80 + 4 * pp;
    const u32 col1 = gm == 0 ? 100 + 4 * pp : gm == 1 ? 4 * pp : gm == 2 ? 20 + 4 * pp : 16 + 20 * (pp & 1);
    const u32 a0 = xl + 30 * K1 + q4 * 240 + col0 * 2, a1 = xl + 30 * K1 + q4 * 240 + col1 * 2;
    waitv<24>();
    ST(26);
    float cs0 = 0.f, cs1 = 0.f;
    sfor<5>([&](auto i) {
      const u32 B = 5 * p + IC(i), ks = B >> 2, gq = B & 3;
      const v4u f0 = tr2(a0 + 2048 * IC(i), a0 + 2048 * IC(i) + 1024), f1 = tr2(a1 + 2048 * IC(i), a1 + 2048 * IC(i) + 1024);
      st16(f0, rws, gm * 1024 + c * 16, WS_WA + g * WA_G + ks * 5120 + gq * 256);
      const u32 o1 = gm == 0 ? ks * 5120 + 4096 + gq * 256 + c * 16
                   : gm < 3  ? 102400 + ks * 2560 + (gm - 1) * 1024 + gq * 256 + c * 16
                             : (c < 8 ? 102400 + ks * 2560 + 2048 + gq * 128 + c * 16 : OOB);
      st16(f1, rws, o1, WS_WA + g * WA_G);
      cs0 += sum8(f0); cs1 += sum8(f1);
    });
    st4(__builtin_bit_cast(u32, cs0), rws, lane * 4, WS_CSA + (g * 16 + p) * 512);
    st4(__builtin_bit_cast(u32, cs1), rws, lane * 4 + 256, WS_CSA + (g * 16 + p) * 512);
  }
  ST(27);
  asm volatile("s_waitcnt lgkmcnt(0)" : "+s"(epoch)::"memory");
  const u32 tag = epoch + 1;
  f32x4 rsr[2], nmr[2]; float rsl[2], nml[2];
  v4u xr0, xr1; v2u xr2;
  v4u ones = {0x3F803F80u, 0x3F803F80u, 0x3F803F80u, 0x3F803F80u};
  asm volatile("" : "+v"(ones));
  {
    const u32 r = c & 7, rb = xl + (c >> 3) * K1 + r * 128;
    const u32 e0 = rb + ((gm ^ (r & 6)) * 16), e1 = rb + (((4 + gm) ^ (r & 6)) * 16);
    const u32 l56 = lane < 56 ? lane : 55, l40 = lane < 40 ? lane : 39, l60 = lane < 60 ? lane : 59;
    const u32 rvo = w < 3 ? (l56 / 14) * 3456 + (l56 % 14) * 16 : (l40 / 10) * 1280 + (l40 % 10) * 16;
    const rsrc_t rd = mkrs(wd_, 1728 * 640 * 2);
    const rsrc_t r1 = w < 3 ? mkrs(w == 1 ? wg_ : wu_, 640 * 1728 * 2) : half ? rd : mkrs(wo_, 640 * 640 * 2);
    const u32 row1 = half ? 16 * (32 + 5 * p) : 40 * p, row2 = half ? row1 + 40 : 32 * p;
    const u32 so1 = w < 3 ? 40 * p * 3456 + g * 224 : row1 * 1280 + n80 * 160, st1 = w < 3 ? 4 * 3456 : 4 * 1280, so2 = row2 * 1280 + n80 * 160;
    f32x4 S[2], G[2];
    sfor<10>([&](auto cb_) {
      constexpr int cb = IC(cb_);
      if constexpr (cb < 4) { sfor<4>([&](auto i) { xpiece(std::integral_constant<int, 24 + 4 * cb + IC(i)>{}); }); waitv<24>(); }
      if constexpr (cb == 3) { if (w == 0) { waitv<16>(); if (lane == 0) st4(tag, rws, 0, myflag); } ST(28); }
      if constexpr (cb == 4) {
        sfor<3>([&](auto i) { dma<1, false>((l60 / 10) * 1280 + (l60 % 10) * 16, rx, (128 * p + 64 * half + 16 * w + 6 * IC(i)) * 1280 + n80 * 160, xl + IC(i) * K1); });
        waitv<20 + 3>();
      }
      if constexpr (cb >= 5) {
        constexpr int j = cb - 5;
        if (w == 0) waitv<4 * (9 - cb) + 3>();
        else if (w < 3) { sfor<2>([&](auto i) { dma<1, false>(rvo, r1, so1 + (2 * j + IC(i)) * st1, xl + (4 + 2 * j + IC(i)) * K1); }); waitv<4 * (9 - cb) + 3 + 2 * (j + 1)>(); }
        else { sfor<2>([&](auto i) { dma<1, false>(rvo, r1, so1 + (2 * j + IC(i)) * st1, xl + (4 + 2 * j + IC(i)) * K1); });
               sfor<2>([&](auto i) { dma<1, false>(rvo, rd, so2 + (2 * j + IC(i)) * 5120, xl + (14 + 2 * j + IC(i)) * K1); }); waitv<4 * (9 - cb) + 3 + 4 * (j + 1)>(); }
      }
      sfor<4>([&](auto i) { constexpr int mt = IC(i) >> 1, ks = 2 * cb + (IC(i) & 1); ds_a<4 * (20 * mt + ks), (cb * 4 + 2 * mt) * 1024>((ks & 1) ? e1 : e0); });
      waitl();
      sfor<4>([&](auto i) { constexpr int mt = IC(i) >> 1, ks = 2 * cb + (IC(i) & 1);
        m16_av<4 * (20 * mt + ks), ks == 0>(S[mt], ones);
        m16_aa<4 * (20 * mt + ks), 4 * (20 * mt + ks), ks == 0>(G[mt]); });
    });
    mfma_done();
    ST(1);
    const u32 sb = xl + 3 * K1;
    for (int m = 0; m < 2; ++m) {
      const float s = pp == 0 ? S[m][0] : pp == 1 ? S[m][1] : pp == 2 ? S[m][2] : S[m][3];
      const float q = pp == 0 ? G[m][0] : pp == 1 ? G[m][1] : pp == 2 ? G[m][2] : G[m][3];
      const float mean = s * (1.f / 640.f), var = fmaxf(q * (1.f / 640.f) - mean * mean, 0.f), rs = __builtin_amdgcn_rsqf(var + 1e-5f);
      if (gm == q4) { lds_st<float>(sb + (m * 16 + c) * 4, rs); lds_st<float>(sb + 128 + (m * 16 + c) * 4, -mean * rs); }
    }
    waitl();
    for (int m = 0; m < 2; ++m) {
      rsr[m] = lds_ld<f32x4>(sb + (m * 16 + 4 * gm) * 4); nmr[m] = lds_ld<f32x4>(sb + 128 + (m * 16 + 4 * gm) * 4);
      rsl[m] = lds_ld<float>(sb + (m * 16 + c) * 4);      nml[m] = lds_ld<float>(sb + 128 + (m * 16 + c) * 4);
    }
    ST(29);
    waitv<0>();
    const u32 xa = xl + (c / 6) * K1 + (c % 6) * 160;
    xr0 = lds_ld<v4u>(xa + gm * 16); xr1 = lds_ld<v4u>(xa + 64 + gm * 16); xr2 = lds_ld<v2u>(xa + 128 + gm * 8);
  }
  if (w == 0) poll(rws, WS_FLG + (c * 16 + g) * 128, tag);
  else if (w < 3) {
    const u32 t1 = gm < 3 ? 4 + gm : 6;
    const u32 a0 = xl + 4 * K1 + q4 * 224 + gm * 32 + pp * 8, a1 = xl + 4 * K1 + q4 * 224 + t1 * 32 + pp * 8;
    float cs0 = 0.f, cs1 = 0.f;
    sfor<5>([&](auto i) {
      const u32 B = 5 * p + IC(i);
      const v4u f0 = tr2(a0 + 2048 * IC(i), a0 + 2048 * IC(i) + 1024), f1 = tr2(a1 + 2048 * IC(i), a1 + 2048 * IC(i) + 1024);
      const u32 so = WS_WC + g * WC_G + (B >> 1) * 7168 + (B & 1) * 512 + (w - 1) * 256;
      st16(f0, rws, gm * 1024 + c * 16, so);
      st16(f1, rws, gm < 3 ? t1 * 1024 + c * 16 : OOB, so);
      cs0 += sum8(f0); cs1 += sum8(f1);
    });
    const u32 so = WS_CSC + (g * 16 + p) * 1024 + (w - 1) * 64;
    st4(__builtin_bit_cast(u32, cs0), rws, gm * 128 + c * 4, so);
    st4(__builtin_bit_cast(u32, cs1), rws, gm < 3 ? t1 * 128 + c * 4 : OOB, so);
  } else {
    const u32 rb = xl + 4 * K1;
    auto set_lo = [&](u32 s) -> u32 {
      if (half) return rb + ((s >> 1) * 4 + (s & 1)) * K1 + q4 * 160;
      if (s < 4) return rb + s * K1 + q4 * 160;
      if (s == 4) return rb + ((q4 & 1) ? 9u : 4u) * K1 + (q4 >> 1) * 160;
      return rb + (10 + ((s - 5) >> 1) * 4 + ((s - 5) & 1)) * K1 + q4 * 160; };
    auto set_d = [&](u32 s) -> u32 { return half ? 2 * K1 : s < 4 ? 5 * K1 : s == 4 ? 320u : 2 * K1; };
    auto set_dst = [&](u32 s) -> u32 {
      if (!half && s < 4) return WS_WB + n80 * 102400 + (p >> 2) * 25600 + (p & 3) * 5120 + s * 256;
      if (!half && s == 4) return WS_WB + n80 * 102400 + (p >> 2) * 25600 + 4 * 5120 + (p & 3) * 256;
      if (!half && s == 9) return OOB;
      const u32 sd = half ? s : s - 5, T = (half ? 32 + 5 * p : 2 * p) + (sd >> 1);
      return T < 108 ? WS_WD + n80 * 286720 + (T >> 1) * 5120 + (2 * (T & 1) + (sd & 1)) * 256 : OOB; };
    const u32 colb = (32 * (gm >> 1) + 8 * pp + 4 * (gm & 1)) * 2;
    for (u32 s = 0; s < 10; ++s) {
      const u32 a = set_lo(s) + colb;
      st16(tr2(a, a + set_d(s)), rws, set_dst(s) + gm * 1024 + c * 16, 0);
    }
    for (u32 i = 0; i < 3; ++i) {
      const u32 s0 = 4 * i + gm, s = s0 < 10 ? s0 : 9;
      const u32 a = set_lo(s) + (64 + 4 * pp) * 2;
      st16(tr2(a, a + set_d(s)), rws, s0 < 10 ? set_dst(s) + 4096 + c * 16 : OOB, 0);
    }
  }
  ST(2);
  BAR();
  ST(3);

  f32x4 kvacc[2][5];
  {
    const u32 wa = WS_WA + g * WA_G;
    auto aop = [&](auto o) {
      constexpr int i = IC(o);
      if constexpr (i < 5) dma_p<i>(l16, rws, wa + w * 5120, L0 + w * 5120);
      else if constexpr (i < 7) dma_p<i - 5>(l16, rws, WS_CSA + g * 8192 + w * 2048, L0 + 152 * K1 + w * 2048);
      else if constexpr (i < 27) dma_p<(i - 7) % 5>(l16, rws, wa + (4 * ((i - 2) / 5) + w) * 5120, L0 + (4 * ((i - 2) / 5) + w) * 5120);
      else if constexpr (i == 27) asm volatile("buffer_load_dword a255, %0, %1, 0 offen sc1" ::"v"(WS_FLG + 5 * 32768 + (c * 16 + 8 * ch + (cc & 3) + 4 * (gm & 1)) * 128), "s"(rws) : "memory");
      else if constexpr (i < 41) dma_p<i - 28>(l16, rws, wa + 102400 + w * 13312, L0 + 102400 + w * 13312);
    };
    constexpr auto cum = [](int s) { return s < 15 ? 12 + s : s < 22 ? 27 : (s - 22 + 27 < 41 ? s - 22 + 27 : 41); };
    sfor<12>(aop);
    v4u wf[2][5];
    waitv<5>(); BAR();
    if (w == 0 && lane == 0) st4(tag, rws, 0, myflag + 5 * 32768);
    for (int t = 0; t < 5; ++t) wf[0][t] = lds_ld<v4u>(LA(t * 1024));
    if (w < 2) {
      float s = 0.f;
      for (int i = 0; i < 16; ++i) s += lds_ld<float>(L0 + 152 * K1 + i * 512 + threadIdx.x * 4);
      lds_st<float>(L0 + 152 * K1 + threadIdx.x * 4, s);
    }
    GAP();
    sfor<20>([&](auto k) {
      constexpr int ks = IC(k);
      if constexpr (ks < 19 && (ks + 1) % 4 == 0) { waitv<cum(2 * ks) - (7 + 5 * ((ks + 1) / 4))>(); BAR(); }
      sfor<5>([&](auto t) {
        if constexpr (ks < 19) wf[(ks + 1) & 1][IC(t)] = lds_ld<v4u>(LA((ks + 1) * 5120 + IC(t) * 1024));
        m16_av<4 * ks, ks == 0>(kvacc[0][IC(t)], wf[ks & 1][IC(t)]);
        if constexpr (IC(t) == 1 || IC(t) == 3) { constexpr int s = 2 * ks + (IC(t) >> 1); if constexpr (cum(s + 1) > cum(s)) aop(std::integral_constant<int, cum(s)>{}); }
        m16_av<4 * (20 + ks), ks == 0>(kvacc[1][IC(t)], wf[ks & 1][IC(t)]);
        GAP();
      });
    });
    mfma_done();
  }
  ST(4);
  constexpr u32 KVS = 86 * 1024;
  {
    v4u T[5];
    for (int t = 0; t < 5; ++t) {
      const float cs = lds_ld<float>(L0 + 152 * K1 + (t * 16 + c) * 4);
      float v[2][4];
      for (int m = 0; m < 2; ++m) for (int r = 0; r < 4; ++r) {
        float y = kvacc[m][t][r] * rsr[m][r] + nmr[m][r] * cs;
        if (t < 2) y = phi(y);
        if (t == 2) y = c < 8 ? phi(y) : y;
        v[m][r] = y;
      }
      T[t] = v4u{cvtpk(v[0][0], v[0][1]), cvtpk(v[0][2], v[0][3]), cvtpk(v[1][0], v[1][1]), cvtpk(v[1][2], v[1][3])};
    }
    const v4u T2b = c < 8 ? ones : T[2];
    f32x4 R[7];
    m16_vv<true>(R[0], T[0], T[3]); m16_vv<true>(R[1], T[1], T[4]); m16_vv<true>(R[2], T[0], T2b); m16_vv<true>(R[3], T[1], T2b);
    m16_vv<true>(R[4], T[2], T[3]); m16_vv<true>(R[5], T[2], T[4]); m16_vv<true>(R[6], T[2], T2b);
    mfma_done();
    const u32 pb = L0 + w * 3456, dmy = 3360;
    const u32 e0 = c == 0 ? 20 : c + 8, e1 = c == 0 ? 20 : c + 4;
    const bool v0 = c == 0 || (c >= 8 && c < 12), v1 = c == 0 || c >= 12;
    lds_st<f32x4>(pb + c * 80 + gm * 16, R[0]);
    lds_st<f32x4>(pb + (21 + c) * 80 + gm * 16, R[1]);
    lds_st<f32x4>(pb + (v0 ? e0 * 80 + gm * 16 : dmy), R[2]);
    lds_st<f32x4>(pb + (v1 ? (21 + e1) * 80 + gm * 16 : dmy), R[3]);
    lds_st<f32x4>(pb + (gm == 0 ? c * 80 + 64 : dmy), R[4]);
    lds_st<f32x4>(pb + (gm == 1 ? (21 + c) * 80 + 64 : dmy), R[5]);
    lds_st<f32x4>(pb + ((gm == 0 && v0) ? e0 * 80 + 64 : (gm == 1 && v1) ? (21 + e1) * 80 + 64 : dmy), R[6]);
  }
  waitv<0>();
  BAR();
  ST(5);
  if (w == 0) {
    for (int i = 0; i < 4; ++i) {
      const u32 idx = i * 64 + lane, a = L0 + (idx < 210 ? idx : 209) * 16;
      const f32x4 s = (lds_ld<f32x4>(a) + lds_ld<f32x4>(a + 3456)) + (lds_ld<f32x4>(a + 2 * 3456) + lds_ld<f32x4>(a + 3 * 3456));
      st16(__builtin_bit_cast(v4u, s), rws, idx < 210 ? idx * 16 : OOB, WS_KVP + (g * 16 + p) * 3456);
    }
  }
  {
    u32 fv;
    asm volatile("v_accvgpr_read_b32 %0, a255" : "=v"(fv));
    if (!__all(fv == tag)) poll(rws, WS_FLG + 5 * 32768 + (c * 16 + 8 * ch + (cc & 3) + 4 * (gm & 1)) * 128, tag);
  }
  ST(6);
  v4u pq0[2], pq1[2];
  const u32 wob = WS_WB + n80 * 102400 + w * 25600;
  {
    f32x4 qa[2][3];
    v4u wf[2][3];
    u32 qb = L0 + 102400 + l16, qb2 = L0 + 102400 + 2048 + gm * 128 + (c & 7) * 16;
    asm volatile("" : "+v"(qb), "+v"(qb2));
    wf[0][0] = lds_ld<v4u>(qb); wf[0][1] = lds_ld<v4u>(qb + 1024); wf[0][2] = lds_ld<v4u>(qb2);
    GAP();
    sfor<20>([&](auto k) {
      constexpr int ks = IC(k);
      if constexpr (ks == 7) { if (w == 0) { waitv<7>(); if (lane == 0) st4(tag, rws, 0, myflag + 32768); } }
      sfor<3>([&](auto t) {
        if constexpr (ks < 19) wf[(ks + 1) & 1][IC(t)] = lds_ld<v4u>((IC(t) < 2 ? qb + IC(t) * 1024 : qb2) + (ks + 1) * 2560);
        m16_va<4 * ks, ks == 0>(qa[0][IC(t)], wf[ks & 1][IC(t)]);
        if constexpr (IC(t) == 1) ld_a<160 + 4 * ks, (ks & 3) * 1024>(l16, rws, wob + (ks >> 2) * 4096);
        m16_va<4 * (20 + ks), ks == 0>(qa[1][IC(t)], wf[ks & 1][IC(t)]);
        GAP();
      });
    });
    mfma_done();
    sfor<4>([&](auto i) { ld_a<240 + 4 * IC(i), IC(i) * 1024>(l16, rws, wob + 5 * 4096); });
    ld_a<156, 0>(l16, rws, wob + 6 * 4096);
    f32x4 cs[3];
    for (int t = 0; t < 3; ++t) cs[t] = lds_ld<f32x4>(L0 + 152 * K1 + ((5 + t) * 16 + 4 * gm) * 4);
    for (int m = 0; m < 2; ++m) {
      u32 pk[3][2];
      for (int t = 0; t < 3; ++t) {
        float v[4];
        for (int r = 0; r < 4; ++r) v[r] = phi(qa[m][t][r] * rsl[m] + nml[m] * cs[t][r]);
        pk[t][0] = cvtpk(v[0], v[1]); pk[t][1] = cvtpk(v[2], v[3]);
      }
      pq0[m] = v4u{pk[0][0], pk[0][1], pk[2][0], pk[2][1]};
      pq1[m] = v4u{pk[1][0], pk[1][1], pk[2][0], pk[2][1]};
    }
  }
  ST(7);
  if (w == 0) poll(rws, WS_FLG + 32768 + (c * 16 + g) * 128, tag);
  BAR();
  ST(8);
  {
    const u32 tid = threadIdx.x, idx = tid < 210 ? tid : 209;
    f32x4 part[16];
    for (int i = 0; i < 16; ++i) part[i] = __builtin_bit_cast(f32x4, __builtin_amdgcn_raw_buffer_load_b128(rws, idx * 16, WS_KVP + (g * 16 + i) * 3456, 16));
    f32x4 s = part[0];
    for (int i = 1; i < 16; ++i) s += part[i];
    lds_st<v2u>(L0 + KVS + idx * 8, v2u{cvtpk(s[0], s[1]), cvtpk(s[2], s[3])});
  }
  BAR();
  ST(9);
  const u32 wcb = WS_WC + g * WC_G;
  {
    const v2u z2 = {0u, 0u};
    const u32 ea = pp == 0 ? 16 + q4 : 20, eb = pp == 1 ? 16 + q4 : 20;
    const bool va = (c & 1) == 0, vb = (c & 1) == 1;
    v2u lo[4], hi[4];
    const u32 rowa[4] = {c, 21 + c, ea, 21 + eb};
    for (int i = 0; i < 4; ++i) { lo[i] = lds_ld<v2u>(L0 + KVS + rowa[i] * 40 + gm * 8); hi[i] = lds_ld<v2u>(L0 + KVS + rowa[i] * 40 + 32); }
    hi[0] = gm == 0 ? hi[0] : z2; hi[1] = gm == 1 ? hi[1] : z2;
    lo[2] = va ? lo[2] : z2; hi[2] = (va && gm == 0) ? hi[2] : z2;
    lo[3] = vb ? lo[3] : z2; hi[3] = (vb && gm == 1) ? hi[3] : z2;
    v4u A[4];
    for (int i = 0; i < 4; ++i) A[i] = v4u{lo[i].x, lo[i].y, hi[i].x, hi[i].y};
    f32x4 O0[2], O1[2], O2[2];
    for (int m = 0; m < 2; ++m) { m16_vv<true>(O0[m], A[0], pq0[m]); m16_vv<true>(O1[m], A[1], pq1[m]); m16_vv<true>(O2[m], A[2], pq0[m]); m16_vv<false>(O2[m], A[3], pq1[m]); }
    mfma_done();
    for (int m = 0; m < 2; ++m) {
      const float r0 = __builtin_amdgcn_rcpf(O2[m][2] + 1e-6f), r1 = __builtin_amdgcn_rcpf(O2[m][3] + 1e-6f);
      const u32 so = WS_O + (8 * p + 2 * w + m) * 20480;
      st16(v4u{cvtpk(O0[m][0] * r0, O0[m][1] * r0), cvtpk(O0[m][2] * r0, O0[m][3] * r0), cvtpk(O1[m][0] * r1, O1[m][1] * r1), cvtpk(O1[m][2] * r1, O1[m][3] * r1)},
           rws, l16, so + g * 1024);
      st4(cvtpk(O2[m][0] * r0, O2[m][1] * r1), rws, c * 16 + gm * 4, so + 16384 + g * 256);
    }
  }
  ST(22);
  dma_lin<7>(l16, rws, wcb + w * 7168, L0 + w * 7168);
  waitv<7>(); BAR();
  ST(10);
  if (w == 0) {
    if (lane == 0) st4(tag, rws, 0, myflag + 2 * 32768);
    poll(rws, WS_FLG + 2 * 32768 + (p * 16 + c) * 128, tag);
  } else {
    if (w == 1) { dma_lin<16>(l16, rws, WS_CSC + g * 16384, L0 + 84 * K1); dma_lin<8>(l16, rws, wcb + 28 * K1, L0 + 28 * K1); waitv<8>(); }
    else dma_lin<24>(l16, rws, wcb + (w * 24 - 12) * K1, L0 + (w * 24 - 12) * K1);
  }
  ST(11);
  BAR();
  ST(12);
  if (b == 0 && threadIdx.x == 0) st4(tag, rws, 0, WS_EPOCH);
  u32 hp[10]; float csv;
  const u32 wdb = WS_WD + n80 * 286720 + w * 71680;
  {
    auto oload = [&](auto i) {
      constexpr int j = IC(i) / 5, k = IC(i) % 5;
      const u32 so = WS_O + (8 * p + 4 * half + ((w + 1 + j) & 3)) * 20480;
      if constexpr (k < 4) ld_a<4 * IC(i), k * 1024>(l16, rws, so + w * 4096);
      else ld_a<4 * IC(i), 0>(l16, rws, so + (16 + w) * 1024);
    };
    constexpr int UP = 9;
    sfor<UP>(oload);
    csv = 0.f;
    for (int i = 0; i < 16; ++i) csv += lds_ld<float>(L0 + 84 * K1 + i * 1024 + threadIdx.x * 4);
    asm volatile("" : "+v"(csv));
    f32x4 acc[4][5];
    const u32 scr = L0 + 100 * K1 + l16;
    sfor<20>([&](auto i) {
      constexpr int j = IC(i) / 5, k = IC(i) % 5, issued = (UP + IC(i) < 20 ? UP + IC(i) : 20);
      waitv<issued - IC(i) - 1>();
      sfor<5>([&](auto t) {
        constexpr int f = 5 * k + IC(t);
        m16_aa<(f < 24 ? 160 + 4 * f : 156), 4 * IC(i), k == 0>(acc[j][IC(t)]);
        if constexpr (IC(t) == 1 && UP + IC(i) < 20) oload(std::integral_constant<int, (UP + IC(i) < 20 ? UP + IC(i) : 0)>{});
        if constexpr (j > 0 && k >= 2 && IC(t) >= 3 && 2 * (k - 2) + IC(t) - 3 < 5) { constexpr int tt = 2 * (k - 2) + IC(t) - 3 < 5 ? 2 * (k - 2) + IC(t) - 3 : 0;
          GAP(); lds_st<f32x4>(scr + ((((w + j) & 3) * 3 + j - 1) * 5 + tt) * K1, acc[j > 0 ? j - 1 : 0][tt]); GAP(); }
      });
    });
    mfma_done();
    ST(13);
    BAR();
    float v[5][4];
    for (int t = 0; t < 5; ++t) {
      f32x4 s = acc[3][t];
      for (int j = 0; j < 3; ++j) s += lds_ld<f32x4>(scr + ((w * 3 + j) * 5 + t) * K1);
      for (int r = 0; r < 4; ++r) v[t][r] = s[r];
    }
    const u32 xr[10] = {xr0.x, xr0.y, xr0.z, xr0.w, xr1.x, xr1.y, xr1.z, xr1.w, xr2.x, xr2.y};
    for (int i = 0; i < 10; ++i) hp[i] = cvtpk(v[i >> 1][2 * (i & 1)] + bflo(xr[i]), v[i >> 1][2 * (i & 1) + 1] + bfhi(xr[i]));
    const u32 so = WS_H + (4 * p + 2 * half + (w >> 1)) * 40960 + 5 * n80 * 1024 + (w & 1) * 256;
    st16(v4u{hp[0], hp[1], hp[2], hp[3]}, rws, gm * 512 + c * 16, so);
    st16(v4u{hp[4], hp[5], hp[6], hp[7]}, rws, gm * 512 + c * 16 + 2048, so);
    st8(v2u{hp[8], hp[9]}, rws, (gm >> 1) * 512 + c * 16 + (gm & 1) * 8 + 4096, so);
    asm volatile("" ::: "memory");
    float s1 = 0.f, s2 = 0.f;
    for (int i = 0; i < 10; ++i) { const float a = bflo(hp[i]), bq = bfhi(hp[i]); s1 += a + bq; s2 += a * a + bq * bq; }
    {
      s1 += __shfl_xor(s1, 16); s2 += __shfl_xor(s2, 16);
      s1 += __shfl_xor(s1, 32); s2 += __shfl_xor(s2, 32);
    }
    st8(v2u{__builtin_bit_cast(u32, s1), __builtin_bit_cast(u32, s2)}, rws, gm == 0 ? c * 8 : OOB, WS_HST + (p * 8 + n80) * 1024 + (64 * half + 16 * w) * 8);
  }
  ST(23);
  auto wdreg = [&](auto i) { ld_a<160 + 4 * IC(i), ((IC(i) + 2) & 3) * 1024>(l16, rws, wdb + ((IC(i) + 46) >> 2) * 4096); };
  sfor<C_WD0>(wdreg);
  waitv<C_WD0>(); BAR();
  ST(14);
  lds_st<float>(L0 + 140 * K1 + threadIdx.x * 4, csv);
  if (w == 0) {
    if (lane == 0) st4(tag, rws, 0, myflag + 3 * 32768);
    poll(rws, WS_FLG + 3 * 32768 + (p * 16 + c) * 128, tag);
  } else {
    if (w == 1) dma_lin<18>(l16, rws, wcb + 84 * K1, L0 + 84 * K1);
    else dma_lin<19>(l16, rws, wcb + (64 + w * 19) * K1, L0 + (64 + w * 19) * K1);
  }
  BAR();
  ST(15);
  {
    auto hload = [&](auto i) { ld_a<4 * IC(i), (IC(i) & 3) * 1024>(l16, rws, WS_H + (4 * p + w) * 40960 + (IC(i) >> 2) * 4096); };
    auto refill = [&](auto i) {
      constexpr int j = IC(i) / 7, q = IC(i) % 7;
      if constexpr (j < 5) dma<1, true>(l16, rws, wcb + (4 * (j + 5) + w) * 7168 + q * 1024, L0 + (4 * j + w) * 7168 + q * 1024);
      else dma<1, true>(l16, rws, wdb + (j - 5) * 7168 + q * 1024, L0 + (4 * (j - 5) + w) * 7168 + q * 1024);
    };
    sfor<2>([&](auto i) { dma<1, true>(gm * 1024 + c * 16, rws, WS_HST + (p * 8 + 4 * IC(i)) * 1024 + w * 256, L0 + 144 * K1 + w * 2048 + IC(i) * 1024); });
    sfor<C_UP>(hload);
    f32x16 acc[7];
    v4u wf[2][7];
    for (int t = 0; t < 7; ++t) wf[0][t] = lds_ld<v4u>(LA(t * 1024));
    GAP();
    sfor<40>([&](auto k) {
      constexpr int ks = IC(k), j = ks / 4;
      if constexpr (ks % 4 == 3) {
        if constexpr (j >= 4 && j < 9) waitv<CP.before[3 * ks] - CP.rlast[j >= 4 ? j - 4 : 0] - 1>();
        BAR();
      }
      waitv<CP.before[3 * ks] - CP.hpos[ks] - 1>();
      sfor<7>([&](auto t) {
        if constexpr (ks < 39) wf[(ks + 1) & 1][IC(t)] = lds_ld<v4u>(LA(((ks + 1) % 20) * 7168 + IC(t) * 1024));
        m32_va<4 * ks, ks == 0>(acc[IC(t)], wf[ks & 1][IC(t)]);
        if constexpr (IC(t) == 1 || IC(t) == 3 || IC(t) == 5) {
          constexpr int s = 3 * ks + (IC(t) >> 1);
          if constexpr (CP.kind[s] == 1) hload(std::integral_constant<int, CP.idx[s]>{});
          if constexpr (CP.kind[s] == 2) refill(std::integral_constant<int, CP.idx[s]>{});
          if constexpr (CP.kind[s] == 3) wdreg(std::integral_constant<int, CP.idx[s]>{});
        }
        GAP();
      });
    });
    mfma_done();
    ST(16);
    const u32 tok = lane & 31, hh = lane >> 5;
    float s1 = 0.f, s2 = 0.f;
    for (int i = 0; i < 8; ++i) { const f32x2 s = lds_ld<f32x2>(L0 + 144 * K1 + w * 2048 + i * 256 + tok * 8); s1 += s[0]; s2 += s[1]; }
    const float mean = s1 * (1.f / 640.f), var = fmaxf(s2 * (1.f / 640.f) - mean * mean, 0.f) + 1e-5f, rs = __builtin_amdgcn_rsqf(var);
    const float ke = rs * -1.4426950408889634f;
    const u32 vo = (tok >> 4) * 57344 + hh * 256 + (tok & 15) * 16, so = WS_ACT + (8 * p + 2 * w) * 57344 + 7 * g * 512;
    u32 csb = L0 + 140 * K1 + hh * 16;
    asm volatile("" : "+v"(csb));
    f32x4 cs[2][4];
    for (int q = 0; q < 4; ++q) cs[0][q] = lds_ld<f32x4>(csb + q * 32);
    sfor<7>([&](auto t_) {
      constexpr int t = IC(t_);
      if constexpr (t < 6) for (int q = 0; q < 4; ++q) cs[(t + 1) & 1][q] = lds_ld<f32x4>(csb + (t + 1) * 128 + q * 32);
      GAP();
      float a[8];
      for (int r = 0; r < 8; ++r) {
        const float gv = acc[t][r] - mean * cs[t & 1][r >> 2][r & 3], uv = acc[t][8 + r] - mean * cs[t & 1][2 + (r >> 2)][r & 3];
        a[r] = (gv * uv) * __builtin_amdgcn_rcpf(__builtin_amdgcn_exp2f(gv * ke) * var + var);
      }
      st16(v4u{cvtpk(a[0], a[1]), cvtpk(a[2], a[3]), cvtpk(a[4], a[5]), cvtpk(a[6], a[7])}, rws, vo, so + t * 512);
    });
    ST(17);
    dma_lin<7>(l16, rws, wdb + 4 * 7168, L0 + (16 + w) * 7168);
  }
  waitv<7>(); BAR();
  ST(18);
  if (w == 0 && lane == 0) st4(tag, rws, 0, myflag + 4 * 32768);
  sfor<11>([&](auto i) { ld_a<116 + 4 * IC(i), ((IC(i) + 3) & 3) * 1024>(l16, rws, wdb + ((IC(i) + 35) >> 2) * 4096); });
  if (w == 0) poll(rws, WS_FLG + 4 * 32768 + (p * 16 + c) * 128, tag);
  BAR();
  ST(19);
  {
    u32 ab[4];
    for (int j = 0; j < 4; ++j) ab[j] = WS_ACT + (8 * p + 4 * half + ((w + 1 + j) & 3)) * 57344 + w * 14336;
    auto aload = [&](auto i) { constexpr int k = IC(i) / 4, j = IC(i) % 4; ld_a<4 * (IC(i) % 28), (k & 3) * 1024>(l16, rws, ab[j] + (k >> 2) * 4096); };
    constexpr int UP = 12;
    sfor<UP>(aload);
    f32x4 acc[4][5];
    v4u wf[2][5];
    u32 wb0 = lb0 + w * 7168, wb1 = wb0 + 57344, wb2 = wb0 + 114688;
    asm volatile("" : "+v"(wb0), "+v"(wb1), "+v"(wb2));
    auto wl = [&](int f) -> u32 { return (f < 14 ? wb0 : f < 28 ? wb1 : wb2) + ((f / 7) & 1) * 28672 + (f % 7) * 1024; };
    waitv<UP>();
    for (int t = 0; t < 5; ++t) wf[0][t] = lds_ld<v4u>(wl(t));
    GAP();
    sfor<14>([&](auto k) {
      constexpr int ks = IC(k), issued = (UP + 4 * ks < 56 ? UP + 4 * ks : 56);
      waitv<issued - 4 * (ks + 1)>();
      if (ks < 12 || w < 3)
        sfor<5>([&](auto t) {
          if constexpr (ks < 6) wf[(ks + 1) & 1][IC(t)] = lds_ld<v4u>(wl(5 * (ks + 1) + IC(t)));
          sfor<4>([&](auto j) {
            if constexpr (ks < 7) m16_va<4 * (4 * ks + IC(j)), ks == 0>(acc[IC(j)][IC(t)], wf[ks & 1][IC(t)]);
            else m16_aa<116 + 4 * (5 * (ks - 7) + IC(t)), 4 * (4 * (ks - 7) + IC(j)), false>(acc[IC(j)][IC(t)]);
            if constexpr (IC(j) == 1 && IC(t) < 4 && UP + 4 * ks + IC(t) < 56) aload(std::integral_constant<int, (UP + 4 * ks + IC(t) < 56 ? UP + 4 * ks + IC(t) : 0)>{});
          });
          GAP();
        });
    });
    mfma_done();
    ST(20);
    for (int j = 0; j < 3; ++j) for (int t = 0; t < 5; ++t) lds_st<f32x4>(L0 + (4 * j + w) * 7168 + t * 1024 + l16, acc[j][t]);
    BAR();
    u32 op[10];
    float v[5][4];
    for (int t = 0; t < 5; ++t) {
      f32x4 s = acc[3][t];
      for (int j = 0; j < 3; ++j) s += lds_ld<f32x4>(L0 + (4 * j + ((w + 3 - j) & 3)) * 7168 + t * 1024 + l16);
      for (int r = 0; r < 4; ++r) v[t][r] = s[r];
    }
    for (int i = 0; i < 10; ++i) op[i] = cvtpk(v[i >> 1][2 * (i & 1)] + bflo(hp[i]), v[i >> 1][2 * (i & 1) + 1] + bfhi(hp[i]));
    const rsrc_t ro = mkrs(out_, 2048 * 640 * 2);
    const u32 oo = (128 * p + 64 * half + 16 * w + c) * 1280 + n80 * 160;
    __builtin_amdgcn_raw_buffer_store_b128(v4u{op[0], op[1], op[2], op[3]}, ro, oo + gm * 16, 0, 0);
    __builtin_amdgcn_raw_buffer_store_b128(v4u{op[4], op[5], op[6], op[7]}, ro, oo + 64 + gm * 16, 0, 0);
    __builtin_amdgcn_raw_buffer_store_b64(v2u{op[8], op[9]}, ro, oo + 128 + gm * 8, 0, 0);
  }
}

extern "C" void kernel_launch(void* const* d_in, const int* in_sizes, int n_in, void* d_out, int out_size, void* d_ws, size_t ws_size,
                              hipStream_t stream) {
  (void)hipFuncSetAttribute((const void*)fused_block, hipFuncAttributeMaxDynamicSharedMemorySize, LDS_BYTES);
  fused_block<<<256, 256, LDS_BYTES, stream>>>(d_in[0], d_in[1], d_in[2], d_in[4], d_in[5], d_in[6], d_out, d_ws);
}
```

```cpp
#include <hip/hip_runtime.h>
#include <utility>

#define DI __device__ __forceinline__
#define LAS __attribute__((address_space(3)))
#define GAS __attribute__((address_space(1)))
typedef unsigned u32;
typedef u32 v4u __attribute__((ext_vector_type(4)));
typedef u32 v2u __attribute__((ext_vector_type(2)));
typedef float f32x2 __attribute__((ext_vector_type(2)));
typedef float f32x4 __attribute__((ext_vector_type(4)));
typedef float f32x16 __attribute__((ext_vector_type(16)));
typedef __bf16 bf16x2 __attribute__((ext_vector_type(2)));
typedef __amdgpu_buffer_rsrc_t rsrc_t;
#define ACLOB "a0","a1","a2","a3","a4","a5","a6","a7","a8","a9","a10","a11","a12","a13","a14","a15","a16","a17","a18","a19","a20","a21","a22","a23","a24","a25","a26","a27","a28","a29","a30","a31","a32","a33","a34","a35","a36","a37","a38","a39","a40","a41","a42","a43","a44","a45","a46","a47","a48","a49","a50","a51","a52","a53","a54","a55","a56","a57","a58","a59","a60","a61","a62","a63","a64","a65","a66","a67","a68","a69","a70","a71","a72","a73","a74","a75","a76","a77","a78","a79","a80","a81","a82","a83","a84","a85","a86","a87","a88","a89","a90","a91","a92","a93","a94","a95","a96","a97","a98","a99","a100","a101","a102","a103","a104","a105","a106","a107","a108","a109","a110","a111","a112","a113","a114","a115","a116","a117","a118","a119","a120","a121","a122","a123","a124","a125","a126","a127","a128","a129","a130","a131","a132","a133","a134","a135","a136","a137","a138","a139","a140","a141","a142","a143","a144","a145","a146","a147","a148","a149","a150","a151","a152","a153","a154","a155","a156","a157","a158","a159","a160","a161","a162","a163","a164","a165","a166","a167","a168","a169","a170","a171","a172","a173","a174","a175","a176","a177","a178","a179","a180","a181","a182","a183","a184","a185","a186","a187","a188","a189","a190","a191","a192","a193","a194","a195","a196","a197","a198","a199","a200","a201","a202","a203","a204","a205","a206","a207","a208","a209","a210","a211","a212","a213","a214","a215","a216","a217","a218","a219","a220","a221","a222","a223","a224","a225","a226","a227","a228","a229","a230","a231","a232","a233","a234","a235","a236","a237","a238","a239","a240","a241","a242","a243","a244","a245","a246","a247","a248","a249","a250","a251","a252","a253","a254","a255"

constexpr u32 WS_EPOCH = 0;
constexpr u32 WS_FLG = 4096;
constexpr u32 WS_WA = WS_FLG + 6 * 32768;
constexpr u32 WA_G = 153600;
constexpr u32 WS_CSA = WS_WA + 16 * WA_G + 4096;
constexpr u32 WS_WB = WS_CSA + 16 * 16 * 512;
constexpr u32 WS_WC = WS_WB + 8 * 102400;
constexpr u32 WC_G = 286720;
constexpr u32 WS_CSC = WS_WC + 16 * WC_G;
constexpr u32 WS_WD = WS_CSC + 16 * 16 * 1024;
constexpr u32 WS_KVP = WS_WD + 8 * 286720;
constexpr u32 WS_O = WS_KVP + 256 * 3456;
constexpr u32 WS_H = WS_O + 128 * 20480;
constexpr u32 WS_HST = WS_H + 64 * 40960;
constexpr u32 WS_ACT = WS_HST + 128 * 1024;
constexpr u32 WS_END = WS_ACT + 128 * 57344;
#define ST(k)
constexpr int LDS_BYTES = 163840;
constexpr u32 CODE_BYTES = 40960;
constexpr u32 K1 = 1024;

template <class F, int... Is> DI void sfor_(F&& f, std::integer_sequence<int, Is...>) { (f(std::integral_constant<int, Is>{}), ...); }
template <int N, class F> DI void sfor(F&& f) { sfor_(f, std::make_integer_sequence<int, N>{}); }
#define IC(x) decltype(x)::value

#define GAP() __builtin_amdgcn_sched_barrier(0)
#define BAR() asm volatile("s_waitcnt lgkmcnt(0)\n\ts_barrier" ::: "memory", ACLOB)
template <int N> DI void waitv() { asm volatile("s_waitcnt vmcnt(%c0)" ::"i"(N > 63 ? 63 : N) : "memory"); }
DI void waitl() { asm volatile("s_waitcnt lgkmcnt(0)" ::: "memory"); }
DI void mfma_done() { GAP(); asm volatile("s_nop 7\n\ts_nop 7" ::: "memory"); GAP(); }
DI void valu_done() { GAP(); asm volatile("s_nop 1" ::: "memory"); GAP(); }

template <int A, int IMM> DI void ld_a(u32 voff, rsrc_t rs, u32 soff) {
  asm volatile("buffer_load_dwordx4 a[%c3:%c4], %0, %1, %2 offen offset:%c5 sc1" ::"v"(voff), "s"(rs), "s"(soff), "i"(A), "i"(A + 3), "i"(IMM) : "memory");
}
template <int N, bool SC1> DI void dma(u32 voff, rsrc_t rs, u32 soff, u32 ldsa) {
#define DMA1(o, s) "\n\tbuffer_load_dwordx4 %0, %1, %2 offen offset:" #o s " lds"
#define DMAH "s_mov_b32 m0, %3\n\ts_nop 0"
#define DMAN(s) \
  if constexpr (N == 1) asm volatile(DMAH DMA1(0, s)::"v"(voff), "s"(rs), "s"(soff), "s"(ldsa) : "memory"); \
  if constexpr (N == 2) asm volatile(DMAH DMA1(0, s) DMA1(1024, s)::"v"(voff), "s"(rs), "s"(soff), "s"(ldsa) : "memory"); \
  if constexpr (N == 3) asm volatile(DMAH DMA1(0, s) DMA1(1024, s) DMA1(2048, s)::"v"(voff), "s"(rs), "s"(soff), "s"(ldsa) : "memory"); \
  if constexpr (N == 4) asm volatile(DMAH DMA1(0, s) DMA1(1024, s) DMA1(2048, s) DMA1(3072, s)::"v"(voff), "s"(rs), "s"(soff), "s"(ldsa) : "memory");
  if constexpr (SC1) { DMAN(" sc1") } else { DMAN("") }
}
template <int N> DI void dma_lin(u32 voff, rsrc_t rs, u32 soff, u32 ldsa) {
  sfor<(N + 3) / 4>([&](auto i) { constexpr int r = (N - 4 * IC(i)) < 4 ? (N - 4 * IC(i)) : 4; dma<r, true>(voff, rs, soff + 4096u * IC(i), ldsa + 4096u * IC(i)); });
}
template <int I> DI void dma_p(u32 voff, rsrc_t rs, u32 soff, u32 ldsa) {
  if constexpr ((I & 3) == 0) dma<1, true>(voff, rs, soff + 1024u * I, ldsa + 1024u * I);
  else asm volatile("buffer_load_dwordx4 %0, %1, %2 offen offset:%c3 sc1 lds" ::"v"(voff), "s"(rs), "s"(soff + 1024u * (I & ~3)), "i"(1024 * (I & 3)) : "memory");
}
template <int A, int IMM> DI void ds_a(u32 addr) {
  asm volatile("ds_read_b128 a[%c1:%c2], %0 offset:%c3" ::"v"(addr), "i"(A), "i"(A + 3), "i"(IMM) : "memory");
}
DI v4u tr2(u32 a0, u32 a1) {
  v2u lo, hi;
  asm volatile("ds_read_b64_tr_b16 %0, %2\n\tds_read_b64_tr_b16 %1, %3\n\ts_waitcnt lgkmcnt(0)" : "=&v"(lo), "=&v"(hi) : "v"(a0), "v"(a1) : "memory");
  return v4u{lo.x, lo.y, hi.x, hi.y};
}
#define M16 "v_mfma_f32_16x16x32_bf16 "
#define M32 "v_mfma_f32_32x32x16_bf16 "
template <int A, bool Z> DI void m16_av(f32x4& c, const v4u& b) {
  if constexpr (Z) asm volatile(M16 "%0, a[%c2:%c3], %1, 0" : "=&v"(c) : "v"(b), "i"(A), "i"(A + 3));
  else asm volatile(M16 "%0, a[%c2:%c3], %1, %0" : "+v"(c) : "v"(b), "i"(A), "i"(A + 3));
}
template <int B, bool Z> DI void m16_va(f32x4& c, const v4u& a) {
  if constexpr (Z) asm volatile(M16 "%0, %1, a[%c2:%c3], 0" : "=&v"(c) : "v"(a), "i"(B), "i"(B + 3));
  else asm volatile(M16 "%0, %1, a[%c2:%c3], %0" : "+v"(c) : "v"(a), "i"(B), "i"(B + 3));
}
template <int A, int B, bool Z> DI void m16_aa(f32x4& c) {
  if constexpr (Z) asm volatile(M16 "%0, a[%c1:%c2], a[%c3:%c4], 0" : "=&v"(c) : "i"(A), "i"(A + 3), "i"(B), "i"(B + 3));
  else asm volatile(M16 "%0, a[%c1:%c2], a[%c3:%c4], %0" : "+v"(c) : "i"(A), "i"(A + 3), "i"(B), "i"(B + 3));
}
template <bool Z> DI void m16_vv(f32x4& c, const v4u& a, const v4u& b) {
  if constexpr (Z) asm volatile("s_nop 1\n\t" M16 "%0, %1, %2, 0" : "=&v"(c) : "v"(a), "v"(b));
  else asm volatile("s_nop 1\n\t" M16 "%0, %1, %2, %0" : "+v"(c) : "v"(a), "v"(b));
}
template <int B, bool Z> DI void m32_va(f32x16& c, const v4u& a) {
  if constexpr (Z) asm volatile(M32 "%0, %1, a[%c2:%c3], 0" : "=&v"(c) : "v"(a), "i"(B), "i"(B + 3));
  else asm volatile(M32 "%0, %1, a[%c2:%c3], %0" : "+v"(c) : "v"(a), "i"(B), "i"(B + 3));
}
DI u32 cvtpk(float lo, float hi) { f32x2 v = {lo, hi}; return __builtin_bit_cast(u32, __builtin_convertvector(v, bf16x2)); }
DI float bflo(u32 u) { return __builtin_bit_cast(float, u << 16); }
DI float bfhi(u32 u) { return __builtin_bit_cast(float, u & 0xffff0000u); }
DI float sum8(v4u v) { return ((bflo(v.x) + bfhi(v.x)) + (bflo(v.y) + bfhi(v.y))) + ((bflo(v.z) + bfhi(v.z)) + (bflo(v.w) + bfhi(v.w))); }
DI float phi(float t) { return (fmaxf(t, 0.f) + __builtin_amdgcn_exp2f(fminf(t, 0.f) * 1.4426950408889634f)) + 1e-6f; }
template <class T> DI T lds_ld(u32 a) { return *(const LAS T*)(size_t)a; }
template <class T> DI void lds_st(u32 a, T v) { *(LAS T*)(size_t)a = v; }
DI void st16(v4u v, rsrc_t rs, u32 voff, u32 soff) {
  asm volatile("buffer_store_dwordx4 %0, %1, %2, %3 offen sc1\n\ts_nop 1" ::"v"(v), "v"(voff), "s"(rs), "s"(soff) : "memory");
}
DI void st8(v2u v, rsrc_t rs, u32 voff, u32 soff) { __builtin_amdgcn_raw_buffer_store_b64(v, rs, voff, soff, 16); }
DI void st4(u32 v, rsrc_t rs, u32 voff, u32 soff) { __builtin_amdgcn_raw_buffer_store_b32(v, rs, voff, soff, 16); }
DI rsrc_t mkrs(const void* p, u32 bytes) { return __builtin_amdgcn_make_buffer_rsrc((void*)p, 0, bytes, 0x00020000); }
constexpr u32 OOB = 0x7ff00000u;

DI void poll(rsrc_t rs, u32 voff, u32 tag) {
#pragma unroll 1
  for (u32 spins = 0; spins < (1u << 16); ++spins) {
    const u32 v = __builtin_amdgcn_raw_buffer_load_b32(rs, voff, 0, 16);
    if (__all(v == tag)) break;
    asm volatile("" ::: "memory");
  }
  asm volatile("" ::: "memory");
}

constexpr int C_UP = 8, C_WD0 = 8;
struct CPlan { int kind[120], idx[120], before[121], hpos[40], rlast[10]; };
constexpr CPlan make_cplan() {
  CPlan P{};
  int nh = C_UP, nr = 0, nf = C_WD0, n = 2 + C_UP;
  for (int i = 0; i < C_UP; ++i) P.hpos[i] = 2 + i;
  for (int s = 0; s < 120; ++s) {
    const int ks = s / 3, pos = s % 3, avail = 7 * ((ks + 1) / 4);
    P.before[s] = n;
    int k = 0;
    if (nh < 40 && pos < 2) k = 1;
    else if (nr < avail && nr < 63) k = 2;
    else if (nf < 24) k = 3;
    else if (nh < 40) k = 1;
    P.kind[s] = k;
    if (k == 1) { P.idx[s] = nh; P.hpos[nh++] = n++; }
    if (k == 2) { P.idx[s] = nr; if (nr % 7 == 6) P.rlast[nr / 7] = n; ++nr; ++n; }
    if (k == 3) { P.idx[s] = nf++; ++n; }
  }
  P.before[120] = n;
  P.rlast[9] = (nr == 63 && nf == 24 && nh == 40) ? 1 : -1;
  return P;
}
constexpr CPlan CP = make_cplan();
static_assert(CP.rlast[9] == 1, "plan incomplete");

__global__ __launch_bounds__(256, 1) void fused_block(const void* __restrict__ x_, const void* __restrict__ wqkv_, const void* __restrict__ wo_,
                                                      const void* __restrict__ wg_, const void* __restrict__ wu_, const void* __restrict__ wd_,
                                                      void* __restrict__ out_, void* ws_, const void* bx_) {
  extern __shared__ __attribute__((aligned(16))) char lds_[];
  const u32 L0 = (u32)(size_t)(LAS char*)lds_;
  ST(0);
  const u32 b = blockIdx.x, xcd = b & 7, r_ = b >> 3;
  const u32 p = (xcd >> 1) * 4 + (r_ >> 3), ch = xcd & 1, cc = r_ & 7, g = 8 * ch + cc, n80 = 4 * ch + (cc & 3), half = cc >> 2;
  const u32 lane = threadIdx.x & 63, w = __builtin_amdgcn_readfirstlane(threadIdx.x >> 6);
  const u32 c = lane & 15, gm = lane >> 4, l16 = lane * 16;
  const rsrc_t rx = mkrs(x_, 2048 * 640 * 2), rws = mkrs(ws_, WS_END);
  u32 epoch;
  asm volatile("s_load_dword %0, %1, 0x0" : "=s"(epoch) : "s"(ws_) : "memory");
  asm volatile("s_nop 4" ::: "memory");
  { const rsrc_t rb = mkrs(bx_, 2048 * 640 * 2);
    asm volatile("buffer_load_dwordx4 a[0:3], %0, %1, %2 offen\n\tbuffer_load_dwordx4 a[4:7], %0, %1, %2 offen offset:1024\n\tbuffer_load_dwordx4 a[8:11], %0, %1, %2 offen offset:2048\n\ts_waitcnt vmcnt(0)" ::"v"(l16), "s"(rb), "s"(b * 10240 + w * 2560) : "memory"); }
  const u32 myflag = WS_FLG + (p * 16 + g) * 128;
  const u32 xl = L0 + w * 40 * K1;

  if (w == 0) {
    const rsrc_t rw = mkrs(wqkv_, 640 * 1920 * 2);
    const u32 l = lane < 60 ? lane : 59, rr = l / 15, cq = l % 15;
    const u32 vo = rr * 3840 + (cq / 5) * 1280 + (cq % 5) * 16;
    sfor<10>([&](auto i) { dma<1, false>(vo, rw, (40 * p + 4 * IC(i)) * 3840 + g * 80, xl + (30 + IC(i)) * K1); });
  } else if (w == 3) {
    unsigned long long pc;
    asm volatile("s_getpc_b64 %0" : "=s"(pc));
    const rsrc_t rc = mkrs((const void*)(pc & ~255ull), CODE_BYTES);
    asm volatile("buffer_load_dwordx4 a[248:251], %0, %1, %2 offen\n\tbuffer_load_dwordx4 a[252:255], %0, %1, %2 offen offset:1024" ::"v"(l16), "s"(rc), "s"(r_ * 2048) : "memory");
  }
  ST(24);
  const u32 xvo = (lane >> 3) * 1280 + (((lane & 7) ^ ((lane >> 3) & 6)) * 16), xso = (128 * p + 32 * w) * 1280;
  auto xpiece = [&](auto i) { dma<1, false>(xvo, rx, xso + (IC(i) & 3) * 10240 + (IC(i) >> 2) * 128, xl + IC(i) * K1); };
  sfor<24>(xpiece);
  ST(25);
  u32 lb0 = L0 + l16, lb1 = lb0 + 61440u, lb2 = lb0 + 122880u;
  asm volatile("" : "+v"(lb0), "+v"(lb1), "+v"(lb2));
  auto LA = [&](u32 off) -> u32 { return (off < 61440u ? lb0 : off < 122880u ? lb1 : lb2) + off % 61440u; };
  const u32 q4 = c >> 2, pp = c & 3;
  if (w == 0) {
    const u32 col0 = gm == 0 ? 40 + 4 * pp : gm == 1 ? 60 + 4 * pp : gm == 2 ? 56 + 20 * pp : 80 + 4 * pp;
    const u32 col1 = gm == 0 ? 100 + 4 * pp : gm == 1 ? 4 * pp : gm == 2 ? 20 + 4 * pp : 16 + 20 * (pp & 1);
    const u32 a0 = xl + 30 * K1 + q4 * 240 + col0 * 2, a1 = xl + 30 * K1 + q4 * 240 + col1 * 2;
    waitv<24>();
    ST(26);
    float cs0 = 0.f, cs1 = 0.f;
    sfor<5>([&](auto i) {
      const u32 B = 5 * p + IC(i), ks = B >> 2, gq = B & 3;
      const v4u f0 = tr2(a0 + 2048 * IC(i), a0 + 2048 * IC(i) + 1024), f1 = tr2(a1 + 2048 * IC(i), a1 + 2048 * IC(i) + 1024);
      st16(f0, rws, gm * 1024 + c * 16, WS_WA + g * WA_G + ks * 5120 + gq * 256);
      const u32 o1 = gm == 0 ? ks * 5120 + 4096 + gq * 256 + c * 16
                   : gm < 3  ? 102400 + ks * 2560 + (gm - 1) * 1024 + gq * 256 + c * 16
                             : (c < 8 ? 102400 + ks * 2560 + 2048 + gq * 128 + c * 16 : OOB);
      st16(f1, rws, o1, WS_WA + g * WA_G);
      cs0 += sum8(f0); cs1 += sum8(f1);
    });
    st4(__builtin_bit_cast(u32, cs0), rws, lane * 4, WS_CSA + (g * 16 + p) * 512);
    st4(__builtin_bit_cast(u32, cs1), rws, lane * 4 + 256, WS_CSA + (g * 16 + p) * 512);
  }
  ST(27);
  asm volatile("s_waitcnt lgkmcnt(0)" : "+s"(epoch)::"memory");
  const u32 tag = epoch + 1;
  f32x4 rsr[2], nmr[2]; float rsl[2], nml[2];
  v4u xr0, xr1; v2u xr2;
  v4u ones = {0x3F803F80u, 0x3F803F80u, 0x3F803F80u, 0x3F803F80u};
  asm volatile("" : "+v"(ones));
  {
    const u32 r = c & 7, rb = xl + (c >> 3) * K1 + r * 128;
    const u32 e0 = rb + ((gm ^ (r & 6)) * 16), e1 = rb + (((4 + gm) ^ (r & 6)) * 16);
    const u32 l56 = lane < 56 ? lane : 55, l40 = lane < 40 ? lane : 39, l60 = lane < 60 ? lane : 59;
    const u32 rvo = w < 3 ? (l56 / 14) * 3456 + (l56 % 14) * 16 : (l40 / 10) * 1280 + (l40 % 10) * 16;
    const rsrc_t rd = mkrs(wd_, 1728 * 640 * 2);
    const rsrc_t r1 = w < 3 ? mkrs(w == 1 ? wg_ : wu_, 640 * 1728 * 2) : half ? rd : mkrs(wo_, 640 * 640 * 2);
    const u32 row1 = half ? 16 * (32 + 5 * p) : 40 * p, row2 = half ? row1 + 40 : 32 * p;
    const u32 so1 = w < 3 ? 40 * p * 3456 + g * 224 : row1 * 1280 + n80 * 160, st1 = w < 3 ? 4 * 3456 : 4 * 1280, so2 = row2 * 1280 + n80 * 160;
    f32x4 S[2], G[2];
    sfor<10>([&](auto cb_) {
      constexpr int cb = IC(cb_);
      if constexpr (cb < 4) { sfor<4>([&](auto i) { xpiece(std::integral_constant<int, 24 + 4 * cb + IC(i)>{}); }); waitv<24>(); }
      if constexpr (cb == 3) { if (w == 0) { waitv<16>(); if (lane == 0) st4(tag, rws, 0, myflag); } ST(28); }
      if constexpr (cb == 4) {
        sfor<3>([&](auto i) { dma<1, false>((l60 / 10) * 1280 + (l60 % 10) * 16, rx, (128 * p + 64 * half + 16 * w + 6 * IC(i)) * 1280 + n80 * 160, xl + IC(i) * K1); });
        waitv<20 + 3>();
      }
      if constexpr (cb >= 5) {
        constexpr int j = cb - 5;
        if (w == 0) waitv<4 * (9 - cb) + 3>();
        else if (w < 3) { sfor<2>([&](auto i) { dma<1, false>(rvo, r1, so1 + (2 * j + IC(i)) * st1, xl + (4 + 2 * j + IC(i)) * K1); }); waitv<4 * (9 - cb) + 3 + 2 * (j + 1)>(); }
        else { sfor<2>([&](auto i) { dma<1, false>(rvo, r1, so1 + (2 * j + IC(i)) * st1, xl + (4 + 2 * j + IC(i)) * K1); });
               sfor<2>([&](auto i) { dma<1, false>(rvo, rd, so2 + (2 * j + IC(i)) * 5120, xl + (14 + 2 * j + IC(i)) * K1); }); waitv<4 * (9 - cb) + 3 + 4 * (j + 1)>(); }
      }
      sfor<4>([&](auto i) { constexpr int mt = IC(i) >> 1, ks = 2 * cb + (IC(i) & 1); ds_a<4 * (20 * mt + ks), (cb * 4 + 2 * mt) * 1024>((ks & 1) ? e1 : e0); });
      waitl();
      sfor<4>([&](auto i) { constexpr int mt = IC(i) >> 1, ks = 2 * cb + (IC(i) & 1);
        m16_av<4 * (20 * mt + ks), ks == 0>(S[mt], ones);
        m16_aa<4 * (20 * mt + ks), 4 * (20 * mt + ks), ks == 0>(G[mt]); });
    });
    mfma_done();
    ST(1);
    const u32 sb = xl + 3 * K1;
    for (int m = 0; m < 2; ++m) {
      const float s = pp == 0 ? S[m][0] : pp == 1 ? S[m][1] : pp == 2 ? S[m][2] : S[m][3];
      const float q = pp == 0 ? G[m][0] : pp == 1 ? G[m][1] : pp == 2 ? G[m][2] : G[m][3];
      const float mean = s * (1.f / 640.f), var = fmaxf(q * (1.f / 640.f) - mean * mean, 0.f), rs = __builtin_amdgcn_rsqf(var + 1e-5f);
      if (gm == q4) { lds_st<float>(sb + (m * 16 + c) * 4, rs); lds_st<float>(sb + 128 + (m * 16 + c) * 4, -mean * rs); }
    }
    waitl();
    for (int m = 0; m < 2; ++m) {
      rsr[m] = lds_ld<f32x4>(sb + (m * 16 + 4 * gm) * 4); nmr[m] = lds_ld<f32x4>(sb + 128 + (m * 16 + 4 * gm) * 4);
      rsl[m] = lds_ld<float>(sb + (m * 16 + c) * 4);      nml[m] = lds_ld<float>(sb + 128 + (m * 16 + c) * 4);
    }
    ST(29);
    waitv<0>();
    const u32 xa = xl + (c / 6) * K1 + (c % 6) * 160;
    xr0 = lds_ld<v4u>(xa + gm * 16); xr1 = lds_ld<v4u>(xa + 64 + gm * 16); xr2 = lds_ld<v2u>(xa + 128 + gm * 8);
  }
  if (w == 0) poll(rws, WS_FLG + (c * 16 + g) * 128, tag);
  else if (w < 3) {
    const u32 t1 = gm < 3 ? 4 + gm : 6;
    const u32 a0 = xl + 4 * K1 + q4 * 224 + gm * 32 + pp * 8, a1 = xl + 4 * K1 + q4 * 224 + t1 * 32 + pp * 8;
    float cs0 = 0.f, cs1 = 0.f;
    sfor<5>([&](auto i) {
      const u32 B = 5 * p + IC(i);
      const v4u f0 = tr2(a0 + 2048 * IC(i), a0 + 2048 * IC(i) + 1024), f1 = tr2(a1 + 2048 * IC(i), a1 + 2048 * IC(i) + 1024);
      const u32 so = WS_WC + g * WC_G + (B >> 1) * 7168 + (B & 1) * 512 + (w - 1) * 256;
      st16(f0, rws, gm * 1024 + c * 16, so);
      st16(f1, rws, gm < 3 ? t1 * 1024 + c * 16 : OOB, so);
      cs0 += sum8(f0); cs1 += sum8(f1);
    });
    const u32 so = WS_CSC + (g * 16 + p) * 1024 + (w - 1) * 64;
    st4(__builtin_bit_cast(u32, cs0), rws, gm * 128 + c * 4, so);
    st4(__builtin_bit_cast(u32, cs1), rws, gm < 3 ? t1 * 128 + c * 4 : OOB, so);
  } else {
    const u32 rb = xl + 4 * K1;
    auto set_lo = [&](u32 s) -> u32 {
      if (half) return rb + ((s >> 1) * 4 + (s & 1)) * K1 + q4 * 160;
      if (s < 4) return rb + s * K1 + q4 * 160;
      if (s == 4) return rb + ((q4 & 1) ? 9u : 4u) * K1 + (q4 >> 1) * 160;
      return rb + (10 + ((s - 5) >> 1) * 4 + ((s - 5) & 1)) * K1 + q4 * 160; };
    auto set_d = [&](u32 s) -> u32 { return half ? 2 * K1 : s < 4 ? 5 * K1 : s == 4 ? 320u : 2 * K1; };
    auto set_dst = [&](u32 s) -> u32 {
      if (!half && s < 4) return WS_WB + n80 * 102400 + (p >> 2) * 25600 + (p & 3) * 5120 + s * 256;
      if (!half && s == 4) return WS_WB + n80 * 102400 + (p >> 2) * 25600 + 4 * 5120 + (p & 3) * 256;
      if (!half && s == 9) return OOB;
      const u32 sd = half ? s : s - 5, T = (half ? 32 + 5 * p : 2 * p) + (sd >> 1);
      return T < 108 ? WS_WD + n80 * 286720 + (T >> 1) * 5120 + (2 * (T & 1) + (sd & 1)) * 256 : OOB; };
    const u32 colb = (32 * (gm >> 1) + 8 * pp + 4 * (gm & 1)) * 2;
    for (u32 s = 0; s < 10; ++s) {
      const u32 a = set_lo(s) + colb;
      st16(tr2(a, a + set_d(s)), rws, set_dst(s) + gm * 1024 + c * 16, 0);
    }
    for (u32 i = 0; i < 3; ++i) {
      const u32 s0 = 4 * i + gm, s = s0 < 10 ? s0 : 9;
      const u32 a = set_lo(s) + (64 + 4 * pp) * 2;
      st16(tr2(a, a + set_d(s)), rws, s0 < 10 ? set_dst(s) + 4096 + c * 16 : OOB, 0);
    }
  }
  ST(2);
  BAR();
  ST(3);

  f32x4 kvacc[2][5];
  {
    const u32 wa = WS_WA + g * WA_G;
    auto aop = [&](auto o) {
      constexpr int i = IC(o);
      if constexpr (i < 5) dma_p<i>(l16, rws, wa + w * 5120, L0 + w * 5120);
      else if constexpr (i < 7) dma_p<i - 5>(l16, rws, WS_CSA + g * 8192 + w * 2048, L0 + 152 * K1 + w * 2048);
      else if constexpr (i < 27) dma_p<(i - 7) % 5>(l16, rws, wa + (4 * ((i - 2) / 5) + w) * 5120, L0 + (4 * ((i - 2) / 5) + w) * 5120);
      else if constexpr (i == 27) asm volatile("buffer_load_dword a255, %0, %1, 0 offen sc1" ::"v"(WS_FLG + 5 * 32768 + (c * 16 + 8 * ch + (cc & 3) + 4 * (gm & 1)) * 128), "s"(rws) : "memory");
      else if constexpr (i < 41) dma_p<i - 28>(l16, rws, wa + 102400 + w * 13312, L0 + 102400 + w * 13312);
    };
    constexpr auto cum = [](int s) { return s < 15 ? 12 + s : s < 22 ? 27 : (s - 22 + 27 < 41 ? s - 22 + 27 : 41); };
    sfor<12>(aop);
    v4u wf[2][5];
    waitv<5>(); BAR();
    if (w == 0 && lane == 0) st4(tag, rws, 0, myflag + 5 * 32768);
    for (int t = 0; t < 5; ++t) wf[0][t] = lds_ld<v4u>(LA(t * 1024));
    if (w < 2) {
      float s = 0.f;
      for (int i = 0; i < 16; ++i) s += lds_ld<float>(L0 + 152 * K1 + i * 512 + threadIdx.x * 4);
      lds_st<float>(L0 + 152 * K1 + threadIdx.x * 4, s);
    }
    GAP();
    sfor<20>([&](auto k) {
      constexpr int ks = IC(k);
      if constexpr (ks < 19 && (ks + 1) % 4 == 0) { waitv<cum(2 * ks) - (7 + 5 * ((ks + 1) / 4))>(); BAR(); }
      sfor<5>([&](auto t) {
        if constexpr (ks < 19) wf[(ks + 1) & 1][IC(t)] = lds_ld<v4u>(LA((ks + 1) * 5120 + IC(t) * 1024));
        m16_av<4 * ks, ks == 0>(kvacc[0][IC(t)], wf[ks & 1][IC(t)]);
        if constexpr (IC(t) == 1 || IC(t) == 3) { constexpr int s = 2 * ks + (IC(t) >> 1); if constexpr (cum(s + 1) > cum(s)) aop(std::integral_constant<int, cum(s)>{}); }
        m16_av<4 * (20 + ks), ks == 0>(kvacc[1][IC(t)], wf[ks & 1][IC(t)]);
        GAP();
      });
    });
    mfma_done();
  }
  ST(4);
  constexpr u32 KVS = 86 * 1024;
  {
    v4u T[5];
    for (int t = 0; t < 5; ++t) {
      const float cs = lds_ld<float>(L0 + 152 * K1 + (t * 16 + c) * 4);
      float v[2][4];
      for (int m = 0; m < 2; ++m) for (int r = 0; r < 4; ++r) {
        float y = kvacc[m][t][r] * rsr[m][r] + nmr[m][r] * cs;
        if (t < 2) y = phi(y);
        if (t == 2) y = c < 8 ? phi(y) : y;
        v[m][r] = y;
      }
      T[t] = v4u{cvtpk(v[0][0], v[0][1]), cvtpk(v[0][2], v[0][3]), cvtpk(v[1][0], v[1][1]), cvtpk(v[1][2], v[1][3])};
    }
    const v4u T2b = c < 8 ? ones : T[2];
    f32x4 R[7];
    m16_vv<true>(R[0], T[0], T[3]); m16_vv<true>(R[1], T[1], T[4]); m16_vv<true>(R[2], T[0], T2b); m16_vv<true>(R[3], T[1], T2b);
    m16_vv<true>(R[4], T[2], T[3]); m16_vv<true>(R[5], T[2], T[4]); m16_vv<true>(R[6], T[2], T2b);
    mfma_done();
    const u32 pb = L0 + w * 3456, dmy = 3360;
    const u32 e0 = c == 0 ? 20 : c + 8, e1 = c == 0 ? 20 : c + 4;
    const bool v0 = c == 0 || (c >= 8 && c < 12), v1 = c == 0 || c >= 12;
    lds_st<f32x4>(pb + c * 80 + gm * 16, R[0]);
    lds_st<f32x4>(pb + (21 + c) * 80 + gm * 16, R[1]);
    lds_st<f32x4>(pb + (v0 ? e0 * 80 + gm * 16 : dmy), R[2]);
    lds_st<f32x4>(pb + (v1 ? (21 + e1) * 80 + gm * 16 : dmy), R[3]);
    lds_st<f32x4>(pb + (gm == 0 ? c * 80 + 64 : dmy), R[4]);
    lds_st<f32x4>(pb + (gm == 1 ? (21 + c) * 80 + 64 : dmy), R[5]);
    lds_st<f32x4>(pb + ((gm == 0 && v0) ? e0 * 80 + 64 : (gm == 1 && v1) ? (21 + e1) * 80 + 64 : dmy), R[6]);
  }
  waitv<0>();
  BAR();
  ST(5);
  if (w == 0) {
    for (int i = 0; i < 4; ++i) {
      const u32 idx = i * 64 + lane, a = L0 + (idx < 210 ? idx : 209) * 16;
      const f32x4 s = (lds_ld<f32x4>(a) + lds_ld<f32x4>(a + 3456)) + (lds_ld<f32x4>(a + 2 * 3456) + lds_ld<f32x4>(a + 3 * 3456));
      st16(__builtin_bit_cast(v4u, s), rws, idx < 210 ? idx * 16 : OOB, WS_KVP + (g * 16 + p) * 3456);
    }
  }
  {
    u32 fv;
    asm volatile("v_accvgpr_read_b32 %0, a255" : "=v"(fv));
    if (!__all(fv == tag)) poll(rws, WS_FLG + 5 * 32768 + (c * 16 + 8 * ch + (cc & 3) + 4 * (gm & 1)) * 128, tag);
  }
  ST(6);
  v4u pq0[2], pq1[2];
  const u32 wob = WS_WB + n80 * 102400 + w * 25600;
  {
    f32x4 qa[2][3];
    v4u wf[2][3];
    u32 qb = L0 + 102400 + l16, qb2 = L0 + 102400 + 2048 + gm * 128 + (c & 7) * 16;
    asm volatile("" : "+v"(qb), "+v"(qb2));
    wf[0][0] = lds_ld<v4u>(qb); wf[0][1] = lds_ld<v4u>(qb + 1024); wf[0][2] = lds_ld<v4u>(qb2);
    GAP();
    sfor<20>([&](auto k) {
      constexpr int ks = IC(k);
      if constexpr (ks == 7) { if (w == 0) { waitv<7>(); if (lane == 0) st4(tag, rws, 0, myflag + 32768); } }
      sfor<3>([&](auto t) {
        if constexpr (ks < 19) wf[(ks + 1) & 1][IC(t)] = lds_ld<v4u>((IC(t) < 2 ? qb + IC(t) * 1024 : qb2) + (ks + 1) * 2560);
        m16_va<4 * ks, ks == 0>(qa[0][IC(t)], wf[ks & 1][IC(t)]);
        if constexpr (IC(t) == 1) ld_a<160 + 4 * ks, (ks & 3) * 1024>(l16, rws, wob + (ks >> 2) * 4096);
        m16_va<4 * (20 + ks), ks == 0>(qa[1][IC(t)], wf[ks & 1][IC(t)]);
        GAP();
      });
    });
    mfma_done();
    sfor<4>([&](auto i) { ld_a<240 + 4 * IC(i), IC(i) * 1024>(l16, rws, wob + 5 * 4096); });
    ld_a<156, 0>(l16, rws, wob + 6 * 4096);
    f32x4 cs[3];
    for (int t = 0; t < 3; ++t) cs[t] = lds_ld<f32x4>(L0 + 152 * K1 + ((5 + t) * 16 + 4 * gm) * 4);
    for (int m = 0; m < 2; ++m) {
      u32 pk[3][2];
      for (int t = 0; t < 3; ++t) {
        float v[4];
        for (int r = 0; r < 4; ++r) v[r] = phi(qa[m][t][r] * rsl[m] + nml[m] * cs[t][r]);
        pk[t][0] = cvtpk(v[0], v[1]); pk[t][1] = cvtpk(v[2], v[3]);
      }
      pq0[m] = v4u{pk[0][0], pk[0][1], pk[2][0], pk[2][1]};
      pq1[m] = v4u{pk[1][0], pk[1][1], pk[2][0], pk[2][1]};
    }
  }
  ST(7);
  if (w == 0) poll(rws, WS_FLG + 32768 + (c * 16 + g) * 128, tag);
  BAR();
  ST(8);
  {
    const u32 tid = threadIdx.x, idx = tid < 210 ? tid : 209;
    f32x4 part[16];
    for (int i = 0; i < 16; ++i) part[i] = __builtin_bit_cast(f32x4, __builtin_amdgcn_raw_buffer_load_b128(rws, idx * 16, WS_KVP + (g * 16 + i) * 3456, 16));
    f32x4 s = part[0];
    for (int i = 1; i < 16; ++i) s += part[i];
    lds_st<v2u>(L0 + KVS + idx * 8, v2u{cvtpk(s[0], s[1]), cvtpk(s[2], s[3])});
  }
  BAR();
  ST(9);
  const u32 wcb = WS_WC + g * WC_G;
  {
    const v2u z2 = {0u, 0u};
    const u32 ea = pp == 0 ? 16 + q4 : 20, eb = pp == 1 ? 16 + q4 : 20;
    const bool va = (c & 1) == 0, vb = (c & 1) == 1;
    v2u lo[4], hi[4];
    const u32 rowa[4] = {c, 21 + c, ea, 21 + eb};
    for (int i = 0; i < 4; ++i) { lo[i] = lds_ld<v2u>(L0 + KVS + rowa[i] * 40 + gm * 8); hi[i] = lds_ld<v2u>(L0 + KVS + rowa[i] * 40 + 32); }
    hi[0] = gm == 0 ? hi[0] : z2; hi[1] = gm == 1 ? hi[1] : z2;
    lo[2] = va ? lo[2] : z2; hi[2] = (va && gm == 0) ? hi[2] : z2;
    lo[3] = vb ? lo[3] : z2; hi[3] = (vb && gm == 1) ? hi[3] : z2;
    v4u A[4];
    for (int i = 0; i < 4; ++i) A[i] = v4u{lo[i].x, lo[i].y, hi[i].x, hi[i].y};
    f32x4 O0[2], O1[2], O2[2];
    for (int m = 0; m < 2; ++m) { m16_vv<true>(O0[m], A[0], pq0[m]); m16_vv<true>(O1[m], A[1], pq1[m]); m16_vv<true>(O2[m], A[2], pq0[m]); m16_vv<false>(O2[m], A[3], pq1[m]); }
    mfma_done();
    for (int m = 0; m < 2; ++m) {
      const float r0 = __builtin_amdgcn_rcpf(O2[m][2] + 1e-6f), r1 = __builtin_amdgcn_rcpf(O2[m][3] + 1e-6f);
      const u32 so = WS_O + (8 * p + 2 * w + m) * 20480;
      st16(v4u{cvtpk(O0[m][0] * r0, O0[m][1] * r0), cvtpk(O0[m][2] * r0, O0[m][3] * r0), cvtpk(O1[m][0] * r1, O1[m][1] * r1), cvtpk(O1[m][2] * r1, O1[m][3] * r1)},
           rws, l16, so + g * 1024);
      st4(cvtpk(O2[m][0] * r0, O2[m][1] * r1), rws, c * 16 + gm * 4, so + 16384 + g * 256);
    }
  }
  ST(22);
  dma_lin<7>(l16, rws, wcb + w * 7168, L0 + w * 7168);
  waitv<7>(); BAR();
  ST(10);
  if (w == 0) {
    if (lane == 0) st4(tag, rws, 0, myflag + 2 * 32768);
    poll(rws, WS_FLG + 2 * 32768 + (p * 16 + c) * 128, tag);
  } else {
    if (w == 1) { dma_lin<16>(l16, rws, WS_CSC + g * 16384, L0 + 84 * K1); dma_lin<8>(l16, rws, wcb + 28 * K1, L0 + 28 * K1); waitv<8>(); }
    else dma_lin<24>(l16, rws, wcb + (w * 24 - 12) * K1, L0 + (w * 24 - 12) * K1);
  }
  ST(11);
  BAR();
  ST(12);
  if (b == 0 && threadIdx.x == 0) st4(tag, rws, 0, WS_EPOCH);
  u32 hp[10]; float csv;
  const u32 wdb = WS_WD + n80 * 286720 + w * 71680;
  {
    auto oload = [&](auto i) {
      constexpr int j = IC(i) / 5, k = IC(i) % 5;
      const u32 so = WS_O + (8 * p + 4 * half + ((w + 1 + j) & 3)) * 20480;
      if constexpr (k < 4) ld_a<4 * IC(i), k * 1024>(l16, rws, so + w * 4096);
      else ld_a<4 * IC(i), 0>(l16, rws, so + (16 + w) * 1024);
    };
    constexpr int UP = 9;
    sfor<UP>(oload);
    csv = 0.f;
    for (int i = 0; i < 16; ++i) csv += lds_ld<float>(L0 + 84 * K1 + i * 1024 + threadIdx.x * 4);
    asm volatile("" : "+v"(csv));
    f32x4 acc[4][5];
    const u32 scr = L0 + 100 * K1 + l16;
    sfor<20>([&](auto i) {
      constexpr int j = IC(i) / 5, k = IC(i) % 5, issued = (UP + IC(i) < 20 ? UP + IC(i) : 20);
      waitv<issued - IC(i) - 1>();
      sfor<5>([&](auto t) {
        constexpr int f = 5 * k + IC(t);
        m16_aa<(f < 24 ? 160 + 4 * f : 156), 4 * IC(i), k == 0>(acc[j][IC(t)]);
        if constexpr (IC(t) == 1 && UP + IC(i) < 20) oload(std::integral_constant<int, (UP + IC(i) < 20 ? UP + IC(i) : 0)>{});
        if constexpr (j > 0 && k >= 2 && IC(t) >= 3 && 2 * (k - 2) + IC(t) - 3 < 5) { constexpr int tt = 2 * (k - 2) + IC(t) - 3 < 5 ? 2 * (k - 2) + IC(t) - 3 : 0;
          GAP(); lds_st<f32x4>(scr + ((((w + j) & 3) * 3 + j - 1) * 5 + tt) * K1, acc[j > 0 ? j - 1 : 0][tt]); GAP(); }
      });
    });
    mfma_done();
    ST(13);
    BAR();
    float v[5][4];
    for (int t = 0; t < 5; ++t) {
      f32x4 s = acc[3][t];
      for (int j = 0; j < 3; ++j) s += lds_ld<f32x4>(scr + ((w * 3 + j) * 5 + t) * K1);
      for (int r = 0; r < 4; ++r) v[t][r] = s[r];
    }
    const u32 xr[10] = {xr0.x, xr0.y, xr0.z, xr0.w, xr1.x, xr1.y, xr1.z, xr1.w, xr2.x, xr2.y};
    for (int i = 0; i < 10; ++i) hp[i] = cvtpk(v[i >> 1][2 * (i & 1)] + bflo(xr[i]), v[i >> 1][2 * (i & 1) + 1] + bfhi(xr[i]));
    const u32 so = WS_H + (4 * p + 2 * half + (w >> 1)) * 40960 + 5 * n80 * 1024 + (w & 1) * 256;
    st16(v4u{hp[0], hp[1], hp[2], hp[3]}, rws, gm * 512 + c * 16, so);
    st16(v4u{hp[4], hp[5], hp[6], hp[7]}, rws, gm * 512 + c * 16 + 2048, so);
    st8(v2u{hp[8], hp[9]}, rws, (gm >> 1) * 512 + c * 16 + (gm & 1) * 8 + 4096, so);
    asm volatile("" ::: "memory");
    float s1 = 0.f, s2 = 0.f;
    for (int i = 0; i < 10; ++i) { const float a = bflo(hp[i]), bq = bfhi(hp[i]); s1 += a + bq; s2 += a * a + bq * bq; }
    {
      s1 += __shfl_xor(s1, 16); s2 += __shfl_xor(s2, 16);
      s1 += __shfl_xor(s1, 32); s2 += __shfl_xor(s2, 32);
    }
    st8(v2u{__builtin_bit_cast(u32, s1), __builtin_bit_cast(u32, s2)}, rws, gm == 0 ? c * 8 : OOB, WS_HST + (p * 8 + n80) * 1024 + (64 * half + 16 * w) * 8);
  }
  ST(23);
  auto wdreg = [&](auto i) { ld_a<160 + 4 * IC(i), ((IC(i) + 2) & 3) * 1024>(l16, rws, wdb + ((IC(i) + 46) >> 2) * 4096); };
  sfor<C_WD0>(wdreg);
  waitv<C_WD0>(); BAR();
  ST(14);
  lds_st<float>(L0 + 140 * K1 + threadIdx.x * 4, csv);
  if (w == 0) {
    if (lane == 0) st4(tag, rws, 0, myflag + 3 * 32768);
    poll(rws, WS_FLG + 3 * 32768 + (p * 16 + c) * 128, tag);
  } else {
    if (w == 1) dma_lin<18>(l16, rws, wcb + 84 * K1, L0 + 84 * K1);
    else dma_lin<19>(l16, rws, wcb + (64 + w * 19) * K1, L0 + (64 + w * 19) * K1);
  }
  BAR();
  ST(15);
  {
    auto hload = [&](auto i) { ld_a<4 * IC(i), (IC(i) & 3) * 1024>(l16, rws, WS_H + (4 * p + w) * 40960 + (IC(i) >> 2) * 4096); };
    auto refill = [&](auto i) {
      constexpr int j = IC(i) / 7, q = IC(i) % 7;
      if constexpr (j < 5) dma<1, true>(l16, rws, wcb + (4 * (j + 5) + w) * 7168 + q * 1024, L0 + (4 * j + w) * 7168 + q * 1024);
      else dma<1, true>(l16, rws, wdb + (j - 5) * 7168 + q * 1024, L0 + (4 * (j - 5) + w) * 7168 + q * 1024);
    };
    sfor<2>([&](auto i) { dma<1, true>(gm * 1024 + c * 16, rws, WS_HST + (p * 8 + 4 * IC(i)) * 1024 + w * 256, L0 + 144 * K1 + w * 2048 + IC(i) * 1024); });
    sfor<C_UP>(hload);
    f32x16 acc[7];
    v4u wf[2][7];
    for (int t = 0; t < 7; ++t) wf[0][t] = lds_ld<v4u>(LA(t * 1024));
    GAP();
    sfor<40>([&](auto k) {
      constexpr int ks = IC(k), j = ks / 4;
      if constexpr (ks % 4 == 3) {
        if constexpr (j >= 4 && j < 9) waitv<CP.before[3 * ks] - CP.rlast[j >= 4 ? j - 4 : 0] - 1>();
        BAR();
      }
      waitv<CP.before[3 * ks] - CP.hpos[ks] - 1>();
      sfor<7>([&](auto t) {
        if constexpr (ks < 39) wf[(ks + 1) & 1][IC(t)] = lds_ld<v4u>(LA(((ks + 1) % 20) * 7168 + IC(t) * 1024));
        m32_va<4 * ks, ks == 0>(acc[IC(t)], wf[ks & 1][IC(t)]);
        if constexpr (IC(t) == 1 || IC(t) == 3 || IC(t) == 5) {
          constexpr int s = 3 * ks + (IC(t) >> 1);
          if constexpr (CP.kind[s] == 1) hload(std::integral_constant<int, CP.idx[s]>{});
          if constexpr (CP.kind[s] == 2) refill(std::integral_constant<int, CP.idx[s]>{});
          if constexpr (CP.kind[s] == 3) wdreg(std::integral_constant<int, CP.idx[s]>{});
        }
        GAP();
      });
    });
    mfma_done();
    ST(16);
    const u32 tok = lane & 31, hh = lane >> 5;
    float s1 = 0.f, s2 = 0.f;
    for (int i = 0; i < 8; ++i) { const f32x2 s = lds_ld<f32x2>(L0 + 144 * K1 + w * 2048 + i * 256 + tok * 8); s1 += s[0]; s2 += s[1]; }
    const float mean = s1 * (1.f / 640.f), var = fmaxf(s2 * (1.f / 640.f) - mean * mean, 0.f) + 1e-5f, rs = __builtin_amdgcn_rsqf(var);
    const float ke = rs * -1.4426950408889634f;
    const u32 vo = (tok >> 4) * 57344 + hh * 256 + (tok & 15) * 16, so = WS_ACT + (8 * p + 2 * w) * 57344 + 7 * g * 512;
    u32 csb = L0 + 140 * K1 + hh * 16;
    asm volatile("" : "+v"(csb));
    f32x4 cs[2][4];
    for (int q = 0; q < 4; ++q) cs[0][q] = lds_ld<f32x4>(csb + q * 32);
    sfor<7>([&](auto t_) {
      constexpr int t = IC(t_);
      if constexpr (t < 6) for (int q = 0; q < 4; ++q) cs[(t + 1) & 1][q] = lds_ld<f32x4>(csb + (t + 1) * 128 + q * 32);
      GAP();
      float a[8];
      for (int r = 0; r < 8; ++r) {
        const float gv = acc[t][r] - mean * cs[t & 1][r >> 2][r & 3], uv = acc[t][8 + r] - mean * cs[t & 1][2 + (r >> 2)][r & 3];
        a[r] = (gv * uv) * __builtin_amdgcn_rcpf(__builtin_amdgcn_exp2f(gv * ke) * var + var);
      }
      st16(v4u{cvtpk(a[0], a[1]), cvtpk(a[2], a[3]), cvtpk(a[4], a[5]), cvtpk(a[6], a[7])}, rws, vo, so + t * 512);
    });
    ST(17);
    dma_lin<7>(l16, rws, wdb + 4 * 7168, L0 + (16 + w) * 7168);
  }
  waitv<7>(); BAR();
  ST(18);
  if (w == 0 && lane == 0) st4(tag, rws, 0, myflag + 4 * 32768);
  sfor<11>([&](auto i) { ld_a<116 + 4 * IC(i), ((IC(i) + 3) & 3) * 1024>(l16, rws, wdb + ((IC(i) + 35) >> 2) * 4096); });
  if (w == 0) poll(rws, WS_FLG + 4 * 32768 + (p * 16 + c) * 128, tag);
  BAR();
  ST(19);
  {
    u32 ab[4];
    for (int j = 0; j < 4; ++j) ab[j] = WS_ACT + (8 * p + 4 * half + ((w + 1 + j) & 3)) * 57344 + w * 14336;
    auto aload = [&](auto i) { constexpr int k = IC(i) / 4, j = IC(i) % 4; ld_a<4 * (IC(i) % 28), (k & 3) * 1024>(l16, rws, ab[j] + (k >> 2) * 4096); };
    constexpr int UP = 12;
    sfor<UP>(aload);
    f32x4 acc[4][5];
    v4u wf[2][5];
    u32 wb0 = lb0 + w * 7168, wb1 = wb0 + 57344, wb2 = wb0 + 114688;
    asm volatile("" : "+v"(wb0), "+v"(wb1), "+v"(wb2));
    auto wl = [&](int f) -> u32 { return (f < 14 ? wb0 : f < 28 ? wb1 : wb2) + ((f / 7) & 1) * 28672 + (f % 7) * 1024; };
    waitv<UP>();
    for (int t = 0; t < 5; ++t) wf[0][t] = lds_ld<v4u>(wl(t));
    GAP();
    sfor<14>([&](auto k) {
      constexpr int ks = IC(k), issued = (UP + 4 * ks < 56 ? UP + 4 * ks : 56);
      waitv<issued - 4 * (ks + 1)>();
      if (ks < 12 || w < 3)
        sfor<5>([&](auto t) {
          if constexpr (ks < 6) wf[(ks + 1) & 1][IC(t)] = lds_ld<v4u>(wl(5 * (ks + 1) + IC(t)));
          sfor<4>([&](auto j) {
            if constexpr (ks < 7) m16_va<4 * (4 * ks + IC(j)), ks == 0>(acc[IC(j)][IC(t)], wf[ks & 1][IC(t)]);
            else m16_aa<116 + 4 * (5 * (ks - 7) + IC(t)), 4 * (4 * (ks - 7) + IC(j)), false>(acc[IC(j)][IC(t)]);
            if constexpr (IC(j) == 1 && IC(t) < 4 && UP + 4 * ks + IC(t) < 56) aload(std::integral_constant<int, (UP + 4 * ks + IC(t) < 56 ? UP + 4 * ks + IC(t) : 0)>{});
          });
          GAP();
        });
    });
    mfma_done();
    ST(20);
    for (int j = 0; j < 3; ++j) for (int t = 0; t < 5; ++t) lds_st<f32x4>(L0 + (4 * j + w) * 7168 + t * 1024 + l16, acc[j][t]);
    BAR();
    u32 op[10];
    float v[5][4];
    for (int t = 0; t < 5; ++t) {
      f32x4 s = acc[3][t];
      for (int j = 0; j < 3; ++j) s += lds_ld<f32x4>(L0 + (4 * j + ((w + 3 - j) & 3)) * 7168 + t * 1024 + l16);
      for (int r = 0; r < 4; ++r) v[t][r] = s[r];
    }
    for (int i = 0; i < 10; ++i) op[i] = cvtpk(v[i >> 1][2 * (i & 1)] + bflo(hp[i]), v[i >> 1][2 * (i & 1) + 1] + bfhi(hp[i]));
    const rsrc_t ro = mkrs(out_, 2048 * 640 * 2);
    const u32 oo = (128 * p + 64 * half + 16 * w + c) * 1280 + n80 * 160;
    __builtin_amdgcn_raw_buffer_store_b128(v4u{op[0], op[1], op[2], op[3]}, ro, oo + gm * 16, 0, 0);
    __builtin_amdgcn_raw_buffer_store_b128(v4u{op[4], op[5], op[6], op[7]}, ro, oo + 64 + gm * 16, 0, 0);
    __builtin_amdgcn_raw_buffer_store_b64(v2u{op[8], op[9]}, ro, oo + 128 + gm * 8, 0, 0);
  }
}

extern "C" void kernel_launch(void* const* d_in, const int* in_sizes, int n_in, void* d_out, int out_size, void* d_ws, size_t ws_size,
                              hipStream_t stream) {
  (void)hipFuncSetAttribute((const void*)fused_block, hipFuncAttributeMaxDynamicSharedMemorySize, LDS_BYTES);
  fused_block<<<256, 256, LDS_BYTES, stream>>>(d_in[0], d_in[1], d_in[2], d_in[4], d_in[5], d_in[6], d_out, d_ws, d_in[3]);
}
```

```cpp
#include <hip/hip_runtime.h>
#include <utility>

#define DI __device__ __forceinline__
#define LAS __attribute__((address_space(3)))
typedef unsigned u32;
typedef u32 v4u __attribute__((ext_vector_type(4)));
typedef u32 v2u __attribute__((ext_vector_type(2)));
typedef float f32x2 __attribute__((ext_vector_type(2)));
typedef float f32x4 __attribute__((ext_vector_type(4)));
typedef float f32x16 __attribute__((ext_vector_type(16)));
typedef __bf16 bf16x2 __attribute__((ext_vector_type(2)));
typedef __amdgpu_buffer_rsrc_t rsrc_t;
#define ACLOB "a0","a1","a2","a3","a4","a5","a6","a7","a8","a9","a10","a11","a12","a13","a14","a15","a16","a17","a18","a19","a20","a21","a22","a23","a24","a25","a26","a27","a28","a29","a30","a31","a32","a33","a34","a35","a36","a37","a38","a39","a40","a41","a42","a43","a44","a45","a46","a47","a48","a49","a50","a51","a52","a53","a54","a55","a56","a57","a58","a59","a60","a61","a62","a63","a64","a65","a66","a67","a68","a69","a70","a71","a72","a73","a74","a75","a76","a77","a78","a79","a80","a81","a82","a83","a84","a85","a86","a87","a88","a89","a90","a91","a92","a93","a94","a95","a96","a97","a98","a99","a100","a101","a102","a103","a104","a105","a106","a107","a108","a109","a110","a111","a112","a113","a114","a115","a116","a117","a118","a119","a120","a121","a122","a123","a124","a125","a126","a127","a128","a129","a130","a131","a132","a133","a134","a135","a136","a137","a138","a139","a140","a141","a142","a143","a144","a145","a146","a147","a148","a149","a150","a151","a152","a153","a154","a155","a156","a157","a158","a159","a160","a161","a162","a163","a164","a165","a166","a167","a168","a169","a170","a171","a172","a173","a174","a175","a176","a177","a178","a179","a180","a181","a182","a183","a184","a185","a186","a187","a188","a189","a190","a191","a192","a193","a194","a195","a196","a197","a198","a199","a200","a201","a202","a203","a204","a205","a206","a207","a208","a209","a210","a211","a212","a213","a214","a215","a216","a217","a218","a219","a220","a221","a222","a223","a224","a225","a226","a227","a228","a229","a230","a231","a232","a233","a234","a235","a236","a237","a238","a239","a240","a241","a242","a243","a244","a245","a246","a247","a248","a249","a250","a251","a252","a253","a254","a255"

constexpr u32 WS_EPOCH = 0;
constexpr u32 WS_FLG = 4096;
constexpr u32 WS_WA = WS_FLG + 8 * 32768;
constexpr u32 WA_G = 153600;
constexpr u32 WS_CSA = WS_WA + 16 * WA_G + 4096;
constexpr u32 WS_WB = WS_CSA + 16 * 16 * 512;
constexpr u32 WS_WC = WS_WB + 8 * 102400;
constexpr u32 WC_G = 286720;
constexpr u32 WS_CSC = WS_WC + 16 * WC_G;
constexpr u32 WS_WD = WS_CSC + 16 * 16 * 1024;
constexpr u32 WS_KVP = WS_WD + 8 * 286720;
constexpr u32 WS_O = WS_KVP + 256 * 3456;
constexpr u32 WS_H = WS_O + 128 * 20480;
constexpr u32 WS_HST = WS_H + 64 * 40960;
constexpr u32 WS_ACT = WS_HST + 128 * 1024;
constexpr u32 WS_END = WS_ACT + 128 * 57344;
#define WC_OFF(g, ks, t) (WS_WC + (size_t)(g) * WC_G + wc_frag(ks, ((g) & 1) ? 6 - (t) : (t)) * 1024)
constexpr int d_kap(int s) { constexpr int o[14] = {0, 6, 7, 13, 1, 5, 8, 12, 2, 4, 9, 11, 3, 10}; return o[s]; }
constexpr unsigned long long d_inv() { unsigned long long v = 0; for (int s = 0; s < 14; ++s) v |= (unsigned long long)s << (4 * d_kap(s)); return v; }
#define WD_OFF(n, ka) (WS_WD + (size_t)(n) * 286720 + ((ka) / 14) * 71680 + ((d_inv() >> (4 * ((ka) % 14))) & 15) * 5120)
constexpr int wd_reg(int f) { return f < 46 ? 4 * (f - 35) : 60 + 4 * (f - 46); }
constexpr int wo_reg(int f) { return f < 23 ? 160 + 4 * f : f == 23 ? 156 : 252; }
constexpr int h_reg(int i) { return i < 15 ? 4 * i : 156 + 4 * (i - 15); }
constexpr int act_reg(int i) { return i % 28 < 4 ? 44 + 4 * (i % 28) : 156 + 4 * (i % 28 - 4); }
constexpr int wc_frag(int ks, int u) { return u < 4 ? 4 * ks + u : u < 6 ? 160 + 2 * ks + u - 4 : 240 + ks; }
#define ST(k)
constexpr int LDS_BYTES = 163840;
constexpr u32 K1 = 1024;
constexpr float LOG2E = 1.4426950408889634f;

template <class F, int... Is> DI void sfor_(F&& f, std::integer_sequence<int, Is...>) { (f(std::integral_constant<int, Is>{}), ...); }
template <int N, class F> DI void sfor(F&& f) { sfor_(f, std::make_integer_sequence<int, N>{}); }
template <int O, class F, int... Is> DI void sfor_o_(F&& f, std::integer_sequence<int, Is...>) { (f(std::integral_constant<int, O + Is>{}), ...); }
template <int O, int N, class F> DI void sfor_o(F&& f) { sfor_o_<O>(f, std::make_integer_sequence<int, N>{}); }
#define IC(x) decltype(x)::value
template <int N> using ic = std::integral_constant<int, N>;

#define GAP() __builtin_amdgcn_sched_barrier(0)
#define BAR() asm volatile("s_waitcnt lgkmcnt(0)\n\ts_barrier" ::: "memory", ACLOB)
#define RBAR() asm volatile("s_barrier" ::: "memory")
#define PIN(x) asm volatile("" : "+v"(x))
template <int N> DI void waitv() { static_assert(N >= 0, "bad count"); asm volatile("s_waitcnt vmcnt(%c0)" ::"i"(N > 63 ? 63 : N) : "memory"); }
DI void waitl() { asm volatile("s_waitcnt lgkmcnt(0)" ::: "memory"); }
DI void mfma_done() { GAP(); asm volatile("s_nop 11" ::: "memory"); GAP(); }

template <int A, int IMM> DI void ld_a(u32 voff, rsrc_t rs, u32 soff) {
  asm volatile("buffer_load_dwordx4 a[%c3:%c4], %0, %1, %2 offen offset:%c5 sc1" ::"v"(voff), "s"(rs), "s"(soff), "i"(A), "i"(A + 3), "i"(IMM) : "memory");
}
DI void ld_v(v4u& d, u32 voff, rsrc_t rs, u32 soff) { asm volatile("buffer_load_dwordx4 %0, %1, %2, %3 offen sc1" : "=v"(d) : "v"(voff), "s"(rs), "s"(soff) : "memory"); }
template <int N, int SC1> DI void dma(u32 voff, rsrc_t rs, u32 soff, u32 ldsa) {
#define DMA1(o, s) "\n\tbuffer_load_dwordx4 %0, %1, %2 offen offset:" #o s " lds"
#define DMAH "s_mov_b32 m0, %3\n\ts_nop 0"
#define DMAN(s) \
  if constexpr (N == 1) asm volatile(DMAH DMA1(0, s)::"v"(voff), "s"(rs), "s"(soff), "s"(ldsa) : "memory"); \
  if constexpr (N == 2) asm volatile(DMAH DMA1(0, s) DMA1(1024, s)::"v"(voff), "s"(rs), "s"(soff), "s"(ldsa) : "memory"); \
  if constexpr (N == 3) asm volatile(DMAH DMA1(0, s) DMA1(1024, s) DMA1(2048, s)::"v"(voff), "s"(rs), "s"(soff), "s"(ldsa) : "memory"); \
  if constexpr (N == 4) asm volatile(DMAH DMA1(0, s) DMA1(1024, s) DMA1(2048, s) DMA1(3072, s)::"v"(voff), "s"(rs), "s"(soff), "s"(ldsa) : "memory");
  if constexpr (SC1 == 1) { DMAN(" sc1") } else if constexpr (SC1 == 0) { DMAN(" nt") } else { DMAN("") }
}
template <int N> DI void dma_lin(u32 voff, rsrc_t rs, u32 soff, u32 ldsa) {
  sfor<(N + 3) / 4>([&](auto i) { constexpr int r = (N - 4 * IC(i)) < 4 ? (N - 4 * IC(i)) : 4; dma<r, true>(voff, rs, soff + 4096u * IC(i), ldsa + 4096u * IC(i)); });
}
template <int I> DI void dma_p(u32 voff, rsrc_t rs, u32 soff, u32 ldsa) {
  if constexpr ((I & 3) == 0) dma<1, true>(voff, rs, soff + 1024u * I, ldsa + 1024u * I);
  else asm volatile("buffer_load_dwordx4 %0, %1, %2 offen offset:%c3 sc1 lds" ::"v"(voff), "s"(rs), "s"(soff + 1024u * (I & ~3)), "i"(1024 * (I & 3)) : "memory");
}
template <int A, int IMM> DI void ds_a(u32 addr) {
  asm volatile("ds_read_b128 a[%c1:%c2], %0 offset:%c3" ::"v"(addr), "i"(A), "i"(A + 3), "i"(IMM) : "memory");
}
template <int IMM> DI void trr(v2u& r, u32 addr) { asm volatile("ds_read_b64_tr_b16 %0, %1 offset:%c2" : "=v"(r) : "v"(addr), "i"(IMM) : "memory"); }
DI void tr_wait10(v2u (&r)[10]) {
  asm volatile("s_waitcnt lgkmcnt(0)" : "+v"(r[0]), "+v"(r[1]), "+v"(r[2]), "+v"(r[3]), "+v"(r[4]), "+v"(r[5]), "+v"(r[6]), "+v"(r[7]), "+v"(r[8]), "+v"(r[9])::"memory");
}
DI v4u cat(v2u a, v2u b) { return v4u{a.x, a.y, b.x, b.y}; }
#define M16 "v_mfma_f32_16x16x32_bf16 "
#define M32 "v_mfma_f32_32x32x16_bf16 "
template <int A, bool Z> DI void m16_av(f32x4& c, const v4u& b) {
  if constexpr (Z) asm volatile(M16 "%0, a[%c2:%c3], %1, 0" : "=&v"(c) : "v"(b), "i"(A), "i"(A + 3));
  else asm volatile(M16 "%0, a[%c2:%c3], %1, %0" : "+v"(c) : "v"(b), "i"(A), "i"(A + 3));
}
template <int B, bool Z> DI void m16_va(f32x4& c, const v4u& a) {
  if constexpr (Z) asm volatile(M16 "%0, %1, a[%c2:%c3], 0" : "=&v"(c) : "v"(a), "i"(B), "i"(B + 3));
  else asm volatile(M16 "%0, %1, a[%c2:%c3], %0" : "+v"(c) : "v"(a), "i"(B), "i"(B + 3));
}
template <int A, int B, bool Z> DI void m16_aa(f32x4& c) {
  if constexpr (Z) asm volatile(M16 "%0, a[%c1:%c2], a[%c3:%c4], 0" : "=&v"(c) : "i"(A), "i"(A + 3), "i"(B), "i"(B + 3));
  else asm volatile(M16 "%0, a[%c1:%c2], a[%c3:%c4], %0" : "+v"(c) : "i"(A), "i"(A + 3), "i"(B), "i"(B + 3));
}
template <bool Z> DI void m16_vv(f32x4& c, const v4u& a, const v4u& b) {
  if constexpr (Z) asm volatile("s_nop 1\n\t" M16 "%0, %1, %2, 0" : "=&v"(c) : "v"(a), "v"(b));
  else asm volatile("s_nop 1\n\t" M16 "%0, %1, %2, %0" : "+v"(c) : "v"(a), "v"(b));
}
template <int B, bool Z> DI void m32_va(f32x16& c, const v4u& a) {
  if constexpr (Z) asm volatile(M32 "%0, %1, a[%c2:%c3], 0" : "=&v"(c) : "v"(a), "i"(B), "i"(B + 3));
  else asm volatile(M32 "%0, %1, a[%c2:%c3], %0" : "+v"(c) : "v"(a), "i"(B), "i"(B + 3));
}
DI void m32_vv(f32x16& c, const v4u& a, const v4u& b) { asm volatile(M32 "%0, %1, %2, %0" : "+v"(c) : "v"(a), "v"(b)); }
DI u32 cvt2(f32x2 v) { return __builtin_bit_cast(u32, __builtin_convertvector(v, bf16x2)); }
DI u32 cvtpk(float lo, float hi) { return cvt2(f32x2{lo, hi}); }
DI float bflo(u32 u) { return __builtin_bit_cast(float, u << 16); }
DI float bfhi(u32 u) { return __builtin_bit_cast(float, u & 0xffff0000u); }
DI u32 fu(float f) { return __builtin_bit_cast(u32, f); }
DI float uf(u32 u) { return __builtin_bit_cast(float, u); }
DI f32x2 lo2(f32x4 v) { return __builtin_shufflevector(v, v, 0, 1); }
DI f32x2 hi2(f32x4 v) { return __builtin_shufflevector(v, v, 2, 3); }
DI f32x2 fma2(f32x2 a, f32x2 b, f32x2 c) { return __builtin_elementwise_fma(a, b, c); }
DI f32x2 bc2(float a) { return f32x2{a, a}; }
DI float dot2(u32 a, u32 b, float c) { return __builtin_amdgcn_fdot2_f32_bf16(__builtin_bit_cast(bf16x2, a), __builtin_bit_cast(bf16x2, b), c, false); }
DI float sum4(v2u v, float c) { return dot2(v.y, 0x3F803F80u, dot2(v.x, 0x3F803F80u, c)); }
DI float exp_le1(float x) { return __builtin_amdgcn_fmed3f(__builtin_amdgcn_exp2f(x), 0.f, 1.f); }
DI f32x2 phi2(f32x2 te) {
  const f32x2 a = te * LOG2E;
  return f32x2{fmaxf(te[0], 1e-6f), fmaxf(te[1], 1e-6f)} + f32x2{exp_le1(a[0]), exp_le1(a[1])};
}
template <class T> DI T lds_ld(u32 a) { return *(const LAS T*)(size_t)a; }
template <class T> DI void lds_st(u32 a, T v) { *(LAS T*)(size_t)a = v; }
DI void st16(v4u v, rsrc_t rs, u32 voff, u32 soff) {
  asm volatile("buffer_store_dwordx4 %0, %1, %2, %3 offen sc1\n\ts_nop 1" ::"v"(v), "v"(voff), "s"(rs), "s"(soff) : "memory");
}
DI void st8(v2u v, rsrc_t rs, u32 voff, u32 soff) { __builtin_amdgcn_raw_buffer_store_b64(v, rs, voff, soff, 16); }
DI void st4(u32 v, rsrc_t rs, u32 voff, u32 soff) { __builtin_amdgcn_raw_buffer_store_b32(v, rs, voff, soff, 16); }
DI rsrc_t mkrs(const void* p, u32 bytes) { return __builtin_amdgcn_make_buffer_rsrc((void*)p, 0, bytes, 0x00020000); }
constexpr u32 OOB = 0x7ff00000u;

DI void poll(rsrc_t rs, u32 voff, u32 soff, u32 tag) {
#pragma unroll 1
  for (u32 spins = 0; spins < (1u << 16); ++spins) {
    const u32 v = __builtin_amdgcn_raw_buffer_load_b32(rs, voff, soff, 16);
    if (__all(v == tag)) break;
    asm volatile("" ::: "memory");
  }
  asm volatile("" ::: "memory");
}

template <int A> DI void flag_check(rsrc_t rs, u32 voff, u32 soff, u32 tag) {
  u32 fv;
  asm volatile("v_accvgpr_read_b32 %0, a%c1" : "=v"(fv) : "i"(A) : "memory");
  if (!__all(fv == tag)) poll(rs, voff, soff, tag);
}
template <int A> DI void flag_load(rsrc_t rs, u32 voff, u32 soff) {
  asm volatile("buffer_load_dword a%c3, %0, %1, %2 offen sc1" ::"v"(voff), "s"(rs), "s"(soff), "i"(A) : "memory");
}

constexpr int C_UP = 8, C_WD0 = 24;
constexpr int CG0 = 5;
struct CPlan { int kind[280], idx[280], before[281], hpos[40], rlast[16], ok; };
constexpr CPlan make_cplan() {
  CPlan P{};
  int nh = C_UP, nr = 0, nf = C_WD0, nl = 0, n = 2 + C_UP;
  for (int i = 0; i < C_UP; ++i) P.hpos[i] = 2 + i;
  for (int f = 0; f < 280; ++f) {
    const int avail = 7 * (f / 28 + 5 - CG0);
    P.before[f] = n;
    int k = 0;
    if (f >= 160 && f % 20 == 19) k = 4;
    else if (f & 1) {
      if (nh < 40 && (f & 3) == 1) k = 1;
      else if (nr < avail) k = 2;
      else if (nf < 24) k = 3;
      else if (nh < 40) k = 1;
      else if (nl < 11 && f > 240 + nl) k = 5;
    }
    P.kind[f] = k;
    if (k == 1) { P.idx[f] = nh; P.hpos[nh++] = n; }
    if (k == 2) { P.idx[f] = nr; if (nr % 7 == 6) P.rlast[CG0 + nr / 7] = n; ++nr; }
    if (k == 3) P.idx[f] = nf++;
    if (k == 5) P.idx[f] = nl++;
    if (k) ++n;
  }
  P.before[280] = n;
  P.ok = (nr == 7 * (14 - CG0) && nf == 24 && nh == 40 && nl == 11);
  return P;
}
constexpr CPlan CP = make_cplan();
static_assert(CP.ok, "plan incomplete");
#define XPREV 2
#define F0V 2
constexpr int XPRE = XPREV, FLAG0_AT = F0V;
#define QKVV 18
#define HEADV 4
constexpr int HEAD = HEADV, Q_F1 = 5, Q_KV = QKVV;
#define RAWPOSV 1
#define STPOSV 0
#define ARDV 16
#define AUPV 12
#define ADV 0
#define AWOV 3
#define QWOV 12
#define XNTV 0
constexpr int A_UP = AUPV, A_DENSE = ADV, A_WO = AWOV, Q_WO = QWOV, XNT = XNTV;
struct APlan { int kind[80], idx[80], before[61], glast[5], nwo, ok; };
constexpr APlan make_aplan() {
  APlan P{};
  int n = 0, ng = 0, nc = 0, nr = 0, nq = 0, nw = 0;
  for (int s = -A_UP; s < 60; ++s) {
    const int ks = s < 0 ? -1 : s / 3;
    if (s >= 0) P.before[s] = n;
    int k = 0;
    if (s >= 0 && s % 3 == 1 && ks >= A_DENSE) k = 0;
    else if (ng == 5 && nc < 2) k = 2;
    else if (ng < 25) k = 1;
    else if (nr < 9) k = 3;
    else if (nq < 13 && ks >= 11) k = 4;
    else if (nw < A_WO) k = 5;
    if (!k) continue;
    P.kind[n] = k;
    if (k == 1) { P.idx[n] = ng; if (ng % 5 == 4) P.glast[ng / 5] = n; ++ng; }
    if (k == 2) P.idx[n] = nc++;
    if (k == 3) P.idx[n] = nr++;
    if (k == 4) P.idx[n] = nq++;
    if (k == 5) P.idx[n] = nw++;
    ++n;
  }
  P.before[60] = n; P.nwo = nw;
  P.ok = ng == 25 && nr == 9 && nq == 13;
  return P;
}
constexpr APlan AP = make_aplan();
static_assert(AP.ok, "plan incomplete");
constexpr int D_UP = 12, C_P1 = 270;
struct DPlan { int kind[96], idx[96], before[85], need[14], chk, ok; };
constexpr DPlan make_dplan() {
  DPlan P{};
  int n = 0, na = 0, n2 = 0, n4 = 0, apos[56] = {}, last2 = 0, fpos = 0;
  P.chk = 5;
  for (int s = -D_UP; s < 84; ++s) {
    const int st = s < 0 ? -1 : s / 6;
    if (s >= 0) P.before[s] = n;
    int k = 0;
    if (st == P.chk - 2 && !n4) k = 4;
    else if (st >= 0 && n2 < 7 && (s & 1)) k = 2;
    else if (na < 56 && na < D_UP + 4 * (st + 1) && (na < 32 || st >= P.chk)) k = 1;
    if (!k) continue;
    P.kind[n] = k;
    if (k == 1) { P.idx[n] = na; apos[na++] = n; }
    if (k == 2) { P.idx[n] = n2++; last2 = n; }
    if (k == 4) { n4 = 1; fpos = n; }
    ++n;
  }
  P.before[84] = n;
  for (int st = 0; st < 14; ++st) {
    int nd = apos[4 * st + 3];
    if (st == 4 && last2 > nd) nd = last2;
    if (st == P.chk && fpos > nd) nd = fpos;
    P.need[st] = nd;
  }
  P.ok = na == 56 && n2 == 7 && n4 == 1;
  for (int st = 0; st < 14; ++st) if (P.need[st] >= P.before[6 * st]) P.ok = 0;
  return P;
}
constexpr DPlan DP = make_dplan();
static_assert(DP.ok, "plan incomplete");
constexpr int R0 = 4;
constexpr int raw_younger(int cb) { int n = 0; for (int s = cb - 3; s < cb; ++s) n += (s >= R0 && s < R0 + 5); return n; }

__global__ __launch_bounds__(256, 1) void fused_block(const void* __restrict__ x_, const void* __restrict__ wqkv_, const void* __restrict__ wo_,
                                                      const void* __restrict__ wg_, const void* __restrict__ wu_, const void* __restrict__ wd_,
                                                      void* __restrict__ out_, void* ws_) {
  extern __shared__ __attribute__((aligned(16))) char lds_[];
  const u32 L0 = (u32)(size_t)(LAS char*)lds_;
  ST(0);
  const u32 b = blockIdx.x, xcd = b & 7, r_ = b >> 3;
  const u32 p = (xcd >> 1) * 4 + (r_ >> 3), ch = xcd & 1, cc = r_ & 7, g = 8 * ch + cc, n80 = 4 * ch + (cc & 3), half = cc >> 2;
  const u32 rot = (((5 * cc) >> 2) + 3 * (r_ >> 3)) % 10;
  const u32 lane = threadIdx.x & 63, w = __builtin_amdgcn_readfirstlane(threadIdx.x >> 6);
  const u32 c = lane & 15, gm = lane >> 4, l16 = lane * 16;
  const rsrc_t rx = mkrs(x_, 2048 * 640 * 2), rws = mkrs(ws_, WS_END);
  asm volatile("s_nop 4" ::: "memory");
  const u32 myflag = WS_FLG + (p * 16 + g) * 128;
  const u32 xl = L0 + (40 + 16 * w) * K1;

  u32 rvo2 = gm * 1280 + (c < 9 ? c : 9) * 16;
  {
    const u32 c14 = c < 14 ? c : 14, sec = (c14 * 13) >> 6, vo = gm * 3840 + sec * 1200 + c14 * 16;
    const rsrc_t rq = mkrs(wqkv_, 640 * 1920 * 2), rw = mkrs(wo_, 640 * 640 * 2);
    const u32 sq = (40 * p + 16 * w) * 3840 + g * 80, sw_ = (40 * p + 12 * half) * 1280 + n80 * 160, ld = L0 + (104 + 4 * w) * K1;
    if (w < 3) { dma<1, 0>(vo, rq, sq, ld); dma<1, 0>(vo, rq, sq + 4 * 3840, ld + K1); }
    else { dma<1, 0>(rvo2, rw, sw_ + 8 * 1280, ld); dma<1, 0>(rvo2, rw, sw_ + 20 * 1280, ld + K1); }
    if (w < 2) { dma<1, 0>(vo, rq, sq + 8 * 3840, ld + 2 * K1); dma<1, 0>(vo, rq, sq + 12 * 3840, ld + 3 * K1); }
    else { const u32 s2 = sw_ + (w - 2) * 24 * 1280; dma<1, 0>(rvo2, rw, s2, ld + 2 * K1); dma<1, 0>(rvo2, rw, s2 + 4 * 1280, ld + 3 * K1); }
  }
  flag_load<247>(rws, 0, WS_EPOCH);
  const u32 xvo = (lane >> 3) * 1280 + (((lane & 7) ^ ((lane >> 3) & 6)) * 16), xso = (128 * p + 32 * w) * 1280;
  auto rblk = [&](u32 cb) -> u32 { const u32 v = cb + rot; return v >= 10 ? v - 10 : v; };
  auto xblock = [&](auto cb) {
    const u32 so = xso + rblk(IC(cb)) * 128;
    sfor<4>([&](auto i) { dma<1, (IC(cb) < XNT ? 0 : 2)>(xvo, rx, so + IC(i) * 10240, xl + ((IC(cb) & 3) * 4 + IC(i)) * K1); });
  };
  sfor<XPRE>(xblock);
  {
    unsigned long long pc; u32 len;
    asm volatile("s_getpc_b64 %0\n.Lfb_pc:\n\ts_mov_b32 %1, .Lfb_end-.Lfb_pc" : "=s"(pc), "=s"(len));
    const rsrc_t rc = mkrs((const void*)(pc & ~255ull), len);
    asm volatile("buffer_load_dwordx2 a[248:249], %0, %1, %2 offen" ::"v"(lane * 8), "s"(rc), "s"((r_ * 4 + w) * 512) : "memory");
  }
  ST(24);
  const u32 q4 = c >> 2, pp = c & 3;
  u32 lb0 = L0 + l16, lb1 = lb0 + 61440u, lb2 = lb0 + 122880u;
  PIN(lb0); PIN(lb1); PIN(lb2);
  auto LA = [&](u32 off) -> u32 { return (off < 61440u ? lb0 : off < 122880u ? lb1 : lb2) + off % 61440u; };
  u32 fvo = gm * 1024 + c * 16;
  PIN(fvo); PIN(rvo2);
  const u32 colb = (32 * (gm >> 1) + 8 * pp + 4 * (gm & 1)) * 2;
  u32 tag, wflag_vo = (c * 16 + 4 * (gm & 1)) * 128;
  const u32 wflag_so = WS_FLG + (8 * ch + (cc & 3)) * 128;
  {
    const u32 tl = 2 * w + (gm & 1);
    const u32 col = tl == 0 ? 40 + 4 * pp : tl == 1 ? 60 + 4 * pp : tl == 2 ? 56 + 20 * pp : tl == 3 ? 80 + 4 * pp : tl == 4 ? 100 + 4 * pp : tl == 5 ? 4 * pp : tl == 6 ? 20 + 4 * pp : 16 + 20 * (pp & 1);
    u32 a0 = L0 + 104 * K1 + (gm >> 1) * 2048 + q4 * 256 + col * 2, d[4];
    const u32 oA = WS_WA + g * WA_G + (tl < 5 ? tl * 1024 : 102400 + (tl - 5) * 1024) + c * 16, oB = tl < 5 ? 5120 : 2560, oC = tl < 7 ? 256 : 128;
    for (int r = 0; r < 3; ++r) { const u32 B = 5 * p + 2 * r + (gm >> 1); d[r] = ((tl == 7 && c >= 8) || (r == 2 && gm >= 2)) ? OOB : oA + (B >> 2) * oB + (B & 3) * oC; }
    const u32 j = w < 3 ? w : (gm < 3 ? gm : 0);
    const bool s4 = half && j == 1;
    u32 a1 = L0 + 114 * K1 + (s4 ? (1 + 3 * (q4 & 1)) * K1 + (q4 >> 1) * 256 : j * K1 + q4 * 256) + (w < 3 ? colb : 128 + 8 * pp), a2 = a1 + (s4 ? 512u : 3 * K1);
    const u32 wb = WS_WB + n80 * 102400 + (p >> 2) * 25600;
    d[3] = (w < 3 ? w : gm) >= 3 - half ? OOB : (s4 ? wb + 4 * 5120 + (p & 3) * 256 : wb + (p & 3) * 5120 + (3 * half + j) * 256) + (w < 3 ? fvo : 4096 + c * 16);
    PIN(a0); PIN(a1); PIN(a2); PIN(wflag_vo);
    for (int i = 0; i < 4; ++i) PIN(d[i]);
    waitv<4 * XPRE + 1>();
    RBAR();
    ST(26);
    v2u r0[10];
    sfor<3>([&](auto r) { trr<4096 * IC(r)>(r0[2 * IC(r)], a0); trr<4096 * IC(r) + 1024>(r0[2 * IC(r) + 1], a0); });
    trr<0>(r0[6], a1); trr<0>(r0[7], a2);
    { u32 e; asm volatile("v_accvgpr_read_b32 %0, a247" : "=v"(e)::"memory"); tag = __builtin_amdgcn_readfirstlane(e) + 1; }
    r0[8] = r0[9] = v2u{0u, 0u};
    tr_wait10(r0);
    for (int i = 0; i < 4; ++i) st16(cat(r0[2 * i], r0[2 * i + 1]), rws, d[i], 0);
    float s01 = sum4(r0[3], sum4(r0[2], sum4(r0[1], sum4(r0[0], 0.f)))), s2 = sum4(r0[5], sum4(r0[4], 0.f));
    u32 sa = fu(s01 + (gm < 2 ? s2 : 0.f)), sb = sa;
    PIN(sb);
    auto sw2 = __builtin_amdgcn_permlane32_swap(sa, sb, false, false);
    st4(fu(uf(sw2[0]) + uf(sw2[1])), rws, lane < 32 ? lane * 4 : OOB, WS_CSA + (g * 16 + p) * 512 + w * 128);
    ST(27);
  }
  sfor_o<XPRE, 3 - XPRE>(xblock);
  u32 rvo1 = gm * 3456 + (c < 13 ? c : 13) * 16;
  auto rawop = [&](auto o) {
    constexpr int i = IC(o);
    if constexpr (i < 5) dma<1, 0>(rvo1, mkrs(w < 2 ? wg_ : wu_, 640 * 1728 * 2), (40 * p + 4 * (5 * (w & 1) + i)) * 3456 + g * 224, L0 + (104 + 5 * w + i) * K1);
    else dma<1, 0>(rvo2, mkrs(wd_, 1728 * 640 * 2), (64 * (2 * p + half) + 16 * w + 4 * (i - 5)) * 1280 + n80 * 160, L0 + (124 + 4 * w + i - 5) * K1);
  };
  u32 ra[2], ro_[12];
  v4u rt[10]; float rcs[2];
  auto rt_read = [&]() {
    v2u r0[10], r1[10];
    if (w == 3) {
      sfor<8>([&](auto s) { constexpr int o = ((IC(s) >> 1) * 4 + (IC(s) & 1)) * 1024; trr<o>(r0[IC(s)], ra[0]); trr<o + 2048>(r1[IC(s)], ra[0]); });
      trr<0>(r0[8], ra[1]); trr<2048>(r1[8], ra[1]); trr<8192>(r0[9], ra[1]); trr<8192 + 2048>(r1[9], ra[1]);
      tr_wait10(r0); tr_wait10(r1);
    } else {
      sfor<10>([&](auto i) { trr<2048 * (IC(i) >> 1)>(r0[IC(i)], ra[IC(i) & 1]); trr<2048 * (IC(i) >> 1) + 1024>(r1[IC(i)], ra[IC(i) & 1]); });
      tr_wait10(r0); tr_wait10(r1);
    }
    for (int i = 0; i < 10; ++i) rt[i] = cat(r0[i], r1[i]);
  };
  auto rt_sum = [&](int i) { rcs[i & 1] = dot2(rt[i].w, 0x3F803F80u, dot2(rt[i].z, 0x3F803F80u, dot2(rt[i].y, 0x3F803F80u, dot2(rt[i].x, 0x3F803F80u, i < 2 ? 0.f : rcs[i & 1])))); };
  auto rt_store = [&](auto i) { if constexpr (IC(i) < 10) st16(rt[IC(i) < 10 ? IC(i) : 0], rws, ro_[IC(i)], 0); else st4(fu(rcs[IC(i) & 1]), rws, ro_[IC(i)], 0); };
  f32x4 rsr[2], nmr[2]; float rsl[2], nml[2];
  v4u xr0, xr1; v2u xr2;
  v4u ones = {0x3F803F80u, 0x3F803F80u, 0x3F803F80u, 0x3F803F80u};
  PIN(ones); PIN(rvo1);
  const u32 wob = WS_WB + n80 * 102400 + w * 25600;
  auto woload = [&](auto i) { ld_a<wo_reg(IC(i)), (IC(i) & 3) * 1024>(l16, rws, wob + (IC(i) >> 2) * 4096); };
  f32x4 kvacc[2][5];
  u32 oa[8];
  constexpr u32 KVS = 86 * 1024;
  {
    const u32 wa = WS_WA + g * WA_G;
    auto rks = [&](u32 ks) -> u32 { const u32 v = ks + 2 * rot; return v >= 20 ? v - 20 : v; };
    auto aop = [&](auto o) {
      constexpr int k = AP.kind[IC(o)], i = AP.idx[IC(o)];
      if constexpr (k == 1) dma_p<i % 5>(l16, rws, wa + rks(4 * (i / 5) + w) * 5120, L0 + (4 * (i / 5) + w) * 5120);
      if constexpr (k == 2) dma_p<i>(l16, rws, WS_CSA + g * 8192 + w * 2048, L0 + 152 * K1 + w * 2048);
      if constexpr (k == 3) rawop(ic<i>{});
      if constexpr (k == 4) { const u32 pc = 4 * i + w, v = pc + 5 * rot; dma<1, 1>(l16, rws, wa + 102400 + (v >= 50 ? v - 50 : v) * K1, L0 + pc * K1); }
      if constexpr (k == 5) woload(ic<i>{});
    };
    const u32 r = c & 7, rb = xl + (c >> 3) * K1 + r * 128;
    u32 e0 = rb + ((gm ^ (r & 6)) * 16), e1 = rb + (((4 + gm) ^ (r & 6)) * 16);
    PIN(e0); PIN(e1);
    f32x4 S[2], G[2];
    auto xstat = [&](auto cb) {
      sfor<4>([&](auto i) { constexpr int mt = IC(i) >> 1, ks = 2 * IC(cb) + (IC(i) & 1);
        m16_av<4 * (20 * mt + ks), ks == 0>(S[mt], ones);
        m16_aa<4 * (20 * mt + ks), 4 * (20 * mt + ks), ks == 0>(G[mt]); }); };
    const u32 l60 = lane < 60 ? lane : 59;
    sfor<10>([&](auto i) {
      constexpr int cb = IC(i), out = cb + 2 < 9 ? cb + 2 : 9;
      if constexpr (cb == FLAG0_AT) { waitv<4 * (out + 1 - XPRE)>(); RBAR(); if (w == 0 && lane == 0) st4(tag, rws, 0, myflag); ST(28); }
      waitv<4 * (out - cb) + (cb > 7 ? 4 : 0) + (cb < XPRE ? 6 : 0)>();
      sfor<4>([&](auto i) { constexpr int mt = IC(i) >> 1, ks = 2 * cb + (IC(i) & 1); ds_a<4 * (20 * mt + ks), ((cb & 3) * 4 + 2 * mt) * 1024>((ks & 1) ? e1 : e0); });
      if constexpr (cb > 0) asm volatile("s_waitcnt lgkmcnt(4)" ::: "memory");
      if constexpr (cb + 3 < 10) xblock(ic<(cb + 3 < 10 ? cb + 3 : 0)>{});
      if constexpr (cb == 7) {
        sfor<3>([&](auto i) { dma<1, 2>((l60 / 10) * 1280 + (l60 % 10) * 16, rx, (128 * p + 64 * half + 16 * w + 6 * IC(i)) * 1280 + n80 * 160, L0 + (140 + 3 * w + IC(i)) * K1); });
        flag_load<255>(rws, wflag_vo, wflag_so);
      }
      if constexpr (cb > 0 && cb < 9) xstat(ic<(cb > 0 ? cb - 1 : 0)>{});
    });
    ST(1);
    waitv<0>();
    flag_check<255>(rws, wflag_vo, wflag_so, tag);
    ST(3);
    sfor<A_UP>(aop);
    waitl(); xstat(ic<8>{}); xstat(ic<9>{});
    {
      const u32 xa = L0 + (140 + 3 * w + c / 6) * K1 + (c % 6) * 160;
      xr0 = lds_ld<v4u>(xa + gm * 16); xr1 = lds_ld<v4u>(xa + 64 + gm * 16); xr2 = lds_ld<v2u>(xa + 128 + gm * 8);
      PIN(xr0); PIN(xr1); PIN(xr2);
    }
    mfma_done();
    for (int m = 0; m < 2; ++m) {
      const float s = pp == 0 ? S[m][0] : pp == 1 ? S[m][1] : pp == 2 ? S[m][2] : S[m][3];
      const float q = pp == 0 ? G[m][0] : pp == 1 ? G[m][1] : pp == 2 ? G[m][2] : G[m][3];
      const float mean = s * (1.f / 640.f), var = fmaxf(q * (1.f / 640.f) - mean * mean, 0.f), rs = __builtin_amdgcn_rsqf(var + 1e-5f);
      if (gm == q4) { lds_st<float>(xl + (m * 16 + c) * 4, rs); lds_st<float>(xl + 128 + (m * 16 + c) * 4, -mean); }
    }
    waitl();
    for (int m = 0; m < 2; ++m) {
      rsr[m] = lds_ld<f32x4>(xl + (m * 16 + 4 * gm) * 4); nmr[m] = lds_ld<f32x4>(xl + 128 + (m * 16 + 4 * gm) * 4);
      rsl[m] = lds_ld<float>(xl + (m * 16 + c) * 4);      nml[m] = lds_ld<float>(xl + 128 + (m * 16 + c) * 4);
      PIN(rsr[m]); PIN(nmr[m]); PIN(rsl[m]); PIN(nml[m]);
    }
    ST(29);
    v4u wf[2][5];
    waitv<A_UP - 5>();
    RBAR();
    ST(25);
    for (int t = 0; t < 5; ++t) wf[0][t] = lds_ld<v4u>(LA(t * 1024));
    GAP();
    sfor<20>([&](auto k) {
      constexpr int ks = IC(k);
      if constexpr (ks < 19 && (ks + 1) % 4 == 0) { waitv<AP.before[3 * ks] - AP.glast[(ks + 1) / 4] - 1>(); RBAR(); }
      sfor<5>([&](auto t) {
        if constexpr (ks < 19) wf[(ks + 1) & 1][IC(t)] = lds_ld<v4u>(LA((ks + 1) * 5120 + IC(t) * 1024));
        m16_av<4 * ks, ks == 0>(kvacc[0][IC(t)], wf[ks & 1][IC(t)]);
        if constexpr ((IC(t) & 1) == 0) { constexpr int s = 3 * ks + (IC(t) >> 1); if constexpr (AP.before[s + 1] > AP.before[s]) aop(ic<AP.before[s]>{}); }
        m16_av<4 * (20 + ks), ks == 0>(kvacc[1][IC(t)], wf[ks & 1][IC(t)]);
        GAP();
      });
      if constexpr (ks == 10) {
        const u32 a = L0 + 152 * K1 + (lane >> 5) * 4096 + w * 128 + (lane & 31) * 4;
        float s = 0.f;
        for (int i = 0; i < 8; ++i) s += lds_ld<float>(a + i * 512);
        lds_st<float>(a, s);
        waitl();
        GAP();
      }
    });
  }
  f32x2 csk[5];
  for (int t = 0; t < 5; ++t) csk[t] = bc2(lds_ld<float>(L0 + 152 * K1 + (t * 16 + c) * 4) + lds_ld<float>(L0 + 156 * K1 + (t * 16 + c) * 4));
  mfma_done();
  ST(4);
  {
    v4u T[5], T2b;
    for (int t = 0; t < 5; ++t) {
      u32 o[4];
      for (int m = 0; m < 2; ++m) for (int h = 0; h < 2; ++h) {
        const f32x2 a = h ? hi2(kvacc[m][t]) : lo2(kvacc[m][t]), nm = h ? hi2(nmr[m]) : lo2(nmr[m]), rs = h ? hi2(rsr[m]) : lo2(rsr[m]);
        const f32x2 d = fma2(nm, csk[t], a);
        if (t < 3) o[2 * m + h] = cvt2(phi2(fma2(d, rs, bc2(1e-6f))));
        if (t > 2) o[2 * m + h] = cvt2(d * rs);
        if (t == 2) T2b[2 * m + h] = c < 8 ? 0x3F803F80u : cvt2(d * rs);
      }
      T[t] = v4u{o[0], o[1], o[2], o[3]};
    }
    f32x4 R[7];
    m16_vv<true>(R[0], T[0], T[3]); m16_vv<true>(R[1], T[1], T[4]); m16_vv<true>(R[2], T[0], T2b); m16_vv<true>(R[3], T[1], T2b);
    m16_vv<true>(R[4], T[2], T[3]); m16_vv<true>(R[5], T[2], T[4]); m16_vv<true>(R[6], T[2], T2b);
    const u32 pb = L0 + 52 * K1 + w * 3456, dmy = 3360;
    const u32 e0 = c == 0 ? 20 : c + 8, e1 = c == 0 ? 20 : c + 4;
    const bool v0 = c == 0 || (c >= 8 && c < 12), v1 = c == 0 || c >= 12;
    u32 ad[6] = {pb + c * 80 + gm * 16, pb + (v0 ? e0 * 80 + gm * 16 : dmy), pb + (v1 ? (21 + e1) * 80 + gm * 16 : dmy), pb + (gm == 0 ? c * 80 + 64 : dmy),
                 pb + (gm == 1 ? (21 + c) * 80 + 64 : dmy), pb + ((gm == 0 && v0) ? e0 * 80 + 64 : (gm == 1 && v1) ? (21 + e1) * 80 + 64 : dmy)};
    for (int i = 0; i < 6; ++i) PIN(ad[i]);
    mfma_done();
    lds_st<f32x4>(ad[0], R[0]); lds_st<f32x4>(ad[0] + 21 * 80, R[1]);
    for (int i = 1; i < 6; ++i) lds_st<f32x4>(ad[i], R[i + 1]);
  }
  waitv<0>();
  BAR();
  ST(5);
  {
    const u32 a = lb0 + 52 * K1 + w * 1024;
    const f32x4 s = (lds_ld<f32x4>(a) + lds_ld<f32x4>(a + 3456)) + (lds_ld<f32x4>(a + 2 * 3456) + lds_ld<f32x4>(a + 3 * 3456));
    st16(__builtin_bit_cast(v4u, s), rws, threadIdx.x < 210 ? l16 : OOB, WS_KVP + (g * 16 + p) * 3456 + w * 1024);
  }
  if (w == 3) {
    const u32 rawl = L0 + 124 * K1, T0 = 4 * (2 * p + half);
    ra[0] = rawl + q4 * 256 + colb; ra[1] = rawl + ((gm >> 1) * 4 + (gm & 1)) * K1 + q4 * 256 + 128 + 8 * pp;
    const u32 ka = T0 >> 1, wd0 = T0 < 112 ? (u32)WD_OFF(n80, ka) : OOB, wd1 = T0 < 112 ? (u32)WD_OFF(n80, ka + 1) : OOB;
    for (u32 s = 0; s < 8; ++s) ro_[s] = (s < 4 ? wd0 : wd1) + (s & 3) * 256 + fvo;
    ro_[8] = wd0 + 4096 + l16; ro_[9] = wd1 + 4096 + l16; ro_[10] = OOB; ro_[11] = OOB;
  } else {
    const u32 t1 = gm < 3 ? 4 + gm : 6, rawl = L0 + (94 + 10 * w) * K1;
    ra[0] = rawl + q4 * 256 + gm * 32 + pp * 8; ra[1] = rawl + q4 * 256 + t1 * 32 + pp * 8;
    const u32 u0 = (g & 1) ? 6 - gm : gm, u1 = (g & 1) ? 6 - t1 : t1, so = WS_WC + g * WC_G + (w - 1) * 256;
    const u32 A0 = (u0 < 4 ? u0 : u0 < 6 ? 156 + u0 : 240) * 1024 + c * 16, B0 = u0 < 4 ? 4096 : u0 < 6 ? 2048 : 1024;
    const u32 A1 = gm < 3 ? (u1 < 4 ? u1 : u1 < 6 ? 156 + u1 : 240) * 1024 + c * 16 : OOB, B1 = u1 < 4 ? 4096 : u1 < 6 ? 2048 : 1024;
    for (int i = 0; i < 5; ++i) { const u32 B = 5 * p + i; ro_[2 * i] = so + A0 + (B >> 1) * B0 + (B & 1) * 512; ro_[2 * i + 1] = so + A1 + (B >> 1) * B1 + (B & 1) * 512; }
    const u32 sc = WS_CSC + (g * 16 + p) * 1024 + (w - 1) * 64;
    ro_[10] = sc + u0 * 128 + c * 4; ro_[11] = gm < 3 ? sc + u1 * 128 + c * 4 : OOB;
  }
  rt_read();
  for (int i = 0; i < 10; ++i) rt_sum(i);
  {
    const u32 tb = L0 + KVS, zr = tb + 1680;
    const u32 ea = pp == 0 ? 16 + q4 : 20, eb = pp == 1 ? 16 + q4 : 20;
    const bool va = (c & 1) == 0, vb = (c & 1) == 1;
    oa[0] = tb + c * 40 + gm * 8;                      oa[1] = gm == 0 ? tb + c * 40 + 32 : zr;
    oa[2] = tb + (21 + c) * 40 + gm * 8;               oa[3] = gm == 1 ? tb + (21 + c) * 40 + 32 : zr;
    oa[4] = va ? tb + ea * 40 + gm * 8 : zr;           oa[5] = (va && gm == 0) ? tb + ea * 40 + 32 : zr;
    oa[6] = vb ? tb + (21 + eb) * 40 + gm * 8 : zr;    oa[7] = (vb && gm == 1) ? tb + (21 + eb) * 40 + 32 : zr;
    for (int i = 0; i < 8; ++i) PIN(oa[i]);
    for (int i = 0; i < 12; ++i) PIN(ro_[i]);
  }
  waitv<0>(); RBAR();
  if (w == 0 && lane == 0) st4(tag, rws, 0, myflag + 32768);
  ST(6);
  v4u pq0[2], pq1[2], part[16];
  {
    f32x4 qa[2][3];
    v4u wf[2][3];
    u32 qb = lb0, qb2 = L0 + 2048 + gm * 128 + (c & 7) * 16, kvo = threadIdx.x < 210 ? threadIdx.x * 16 : OOB;
    PIN(qb); PIN(qb2); PIN(kvo);
    wf[0][0] = lds_ld<v4u>(qb); wf[0][1] = lds_ld<v4u>(qb + 1024); wf[0][2] = lds_ld<v4u>(qb2);
    GAP();
    auto kvload = [&](auto i) { ld_v(part[IC(i)], kvo, rws, WS_KVP + (g * 16 + IC(i)) * 3456); };
    auto hop1 = [&]() {
      ST(7);
      waitv<0>(); RBAR();
      if (w == 0 && lane == 0) st4(tag, rws, 0, myflag + 5 * 32768);
      flag_check<254>(rws, c * 2048, WS_FLG + 32768 + g * 128, tag);
      ST(8); };
    sfor<20>([&](auto k) {
      constexpr int ks = IC(k);
      if constexpr (ks == Q_KV - 4) flag_load<254>(rws, c * 2048, WS_FLG + 32768 + g * 128);
      if constexpr (ks == Q_KV) hop1();
      sfor<3>([&](auto t) {
        if constexpr (ks < 19) wf[(ks + 1) & 1][IC(t)] = lds_ld<v4u>((IC(t) < 2 ? qb + IC(t) * 1024 : qb2) + (ks + 1) * 2560);
        m16_va<4 * ks, ks == 0>(qa[0][IC(t)], wf[ks & 1][IC(t)]);
        constexpr int li = 3 * (ks - Q_KV) + IC(t);
        if constexpr (ks < Q_WO && IC(t) == 2) woload(ic<AP.nwo + (ks < Q_WO ? ks : 0)>{});
        if constexpr (ks < 12 && IC(t) == 0) { if (w) rt_store(ic<(ks < 12 ? ks : 0)>{}); }
        if constexpr (ks >= Q_KV && li < 16) kvload(ic<(li >= 0 && li < 16 ? li : 0)>{});
        m16_va<4 * (20 + ks), ks == 0>(qa[1][IC(t)], wf[ks & 1][IC(t)]);
        GAP();
      });
    });
    if constexpr (Q_KV == 20) hop1();
    sfor<16>([&](auto i) { if constexpr (IC(i) >= 3 * (20 - Q_KV)) kvload(i); });
    sfor_o<AP.nwo + Q_WO, 24 - AP.nwo - Q_WO>(woload);
    f32x4 cs[3];
    for (int t = 0; t < 3; ++t) cs[t] = lds_ld<f32x4>(L0 + 152 * K1 + ((5 + t) * 16 + 4 * gm) * 4) + lds_ld<f32x4>(L0 + 156 * K1 + ((5 + t) * 16 + 4 * gm) * 4);
    mfma_done();
    for (int m = 0; m < 2; ++m) {
      u32 pk[3][2];
      for (int t = 0; t < 3; ++t) {
        pk[t][0] = cvt2(phi2(fma2(fma2(bc2(nml[m]), lo2(cs[t]), lo2(qa[m][t])), bc2(rsl[m]), bc2(1e-6f))));
        pk[t][1] = cvt2(phi2(fma2(fma2(bc2(nml[m]), hi2(cs[t]), hi2(qa[m][t])), bc2(rsl[m]), bc2(1e-6f))));
      }
      pq0[m] = v4u{pk[0][0], pk[0][1], pk[2][0], pk[2][1]};
      pq1[m] = v4u{pk[1][0], pk[1][1], pk[2][0], pk[2][1]};
    }
    for (int m = 0; m < 2; ++m) { PIN(pq0[m]); PIN(pq1[m]); }
  }
  {
    waitv<24 - AP.nwo - Q_WO>();
    asm volatile("; kv partials landed" : "+v"(part[0]), "+v"(part[1]), "+v"(part[2]), "+v"(part[3]), "+v"(part[4]), "+v"(part[5]), "+v"(part[6]), "+v"(part[7]),
                 "+v"(part[8]), "+v"(part[9]), "+v"(part[10]), "+v"(part[11]), "+v"(part[12]), "+v"(part[13]), "+v"(part[14]), "+v"(part[15])::"memory");
    f32x4 s = __builtin_bit_cast(f32x4, part[0]);
    for (int i = 1; i < 16; ++i) s += __builtin_bit_cast(f32x4, part[i]);
    lds_st<v2u>(L0 + KVS + threadIdx.x * 8, v2u{cvt2(lo2(s)), cvt2(hi2(s))});
  }
  flag_load<255>(rws, wflag_vo, wflag_so + 5 * 32768);
  BAR();
  ST(9);
  const u32 wcb = WS_WC + g * WC_G, wdb = WS_WD + n80 * 286720 + w * 71680;
  auto wdreg = [&](auto i) { ld_a<60 + 4 * IC(i), ((IC(i) + 2) & 3) * 1024>(l16, rws, wdb + ((IC(i) + 46) >> 2) * 4096); };
  {
    v4u A[4];
    for (int i = 0; i < 4; ++i) A[i] = cat(lds_ld<v2u>(oa[2 * i]), lds_ld<v2u>(oa[2 * i + 1]));
    f32x4 O0[2], O1[2], O2[2];
    for (int m = 0; m < 2; ++m) { m16_vv<true>(O2[m], A[2], pq0[m]); m16_vv<false>(O2[m], A[3], pq1[m]); }
    for (int m = 0; m < 2; ++m) { m16_vv<true>(O0[m], A[0], pq0[m]); m16_vv<true>(O1[m], A[1], pq1[m]); }
    mfma_done();
    for (int m = 0; m < 2; ++m) {
      const f32x2 dn = hi2(O2[m]) + 1e-6f, r = {__builtin_amdgcn_rcpf(dn[0]), __builtin_amdgcn_rcpf(dn[1])};
      const u32 so = WS_O + (8 * p + 2 * w + m) * 20480;
      st16(v4u{cvt2(lo2(O0[m]) * r[0]), cvt2(hi2(O0[m]) * r[0]), cvt2(lo2(O1[m]) * r[1]), cvt2(hi2(O1[m]) * r[1])}, rws, l16, so + g * 1024);
      st4(cvt2(lo2(O2[m]) * r), rws, c * 16 + gm * 4, so + 16384 + g * 256);
    }
  }
  ST(22);
  waitv<4>();
  flag_check<255>(rws, wflag_vo, wflag_so + 5 * 32768, tag);
  woload(ic<24>{});
  sfor<7>(wdreg);
  waitv<8>(); BAR();
  ST(10);
  if (w == 0 && lane == 0) st4(tag, rws, 0, myflag + 2 * 32768);
  sfor_o<7, C_WD0 - 7>(wdreg);
  f32x4 bacc[4][5];
  {
    const u32 xr[10] = {xr0.x, xr0.y, xr0.z, xr0.w, xr1.x, xr1.y, xr1.z, xr1.w, xr2.x, xr2.y};
    for (int t = 0; t < 5; ++t) { bacc[3][t] = f32x4{bflo(xr[2 * t]), bfhi(xr[2 * t]), bflo(xr[2 * t + 1]), bfhi(xr[2 * t + 1])}; PIN(bacc[3][t]); }
  }
  ST(11);
  poll(rws, (c & 3) * 128, WS_FLG + 2 * 32768 + p * 2048 + w * 512, tag);
  ST(12);
  u32 hp[10]; float csv;
  {
    auto oload = [&](auto i) {
      constexpr int j = IC(i) / 5, k = IC(i) % 5;
      const u32 so = WS_O + (8 * p + 4 * half + ((w + 1 + j) & 3)) * 20480;
      if constexpr (k < 4) ld_a<4 * (IC(i) % 15), k * 1024>(l16, rws, so + w * 4096);
      else ld_a<4 * (IC(i) % 15), 0>(l16, rws, so + (16 + w) * 1024);
    };
    constexpr int UP = 9;
    sfor<UP>(oload);
    auto bop = [&](auto i) {
      if constexpr (IC(i) < 4) dma_p<IC(i)>(l16, rws, WS_CSC + g * 16384 + w * 4096, L0 + 84 * K1 + w * 4096);
      else dma_p<(IC(i) - 4) % 7>(l16, rws, wcb + (4 * ((IC(i) - 4) / 7) + w) * 7168, L0 + (4 * ((IC(i) - 4) / 7) + w) * 7168); };
    u32 sw[3], sr = L0 + 100 * K1 + l16 + w * 15 * K1;
    for (int j = 0; j < 3; ++j) { sw[j] = L0 + 100 * K1 + l16 + (((w + 1 + j) & 3) * 3 + j) * 5 * K1; PIN(sw[j]); }
    PIN(sr);
    sfor<20>([&](auto i) {
      constexpr int j = IC(i) / 5, k = IC(i) % 5, issued = (UP + IC(i) < 20 ? UP + IC(i) : 20 + 2 * (UP + IC(i) - 20));
      waitv<issued - IC(i) - 1>();
      sfor<5>([&](auto t) {
        constexpr int f = 5 * k + IC(t), tt = 2 * (k - 2) + IC(t) - 3;
        m16_aa<wo_reg(f), 4 * (IC(i) % 15), (k == 0 && j < 3)>(bacc[j][IC(t)]);
        if constexpr (IC(t) == 1 && UP + IC(i) < 20) oload(ic<(UP + IC(i) < 20 ? UP + IC(i) : 0)>{});
        if constexpr ((IC(t) == 0 || IC(t) == 2) && UP + IC(i) >= 20) bop(ic<(UP + IC(i) >= 20 ? 2 * (UP + IC(i) - 20) + IC(t) / 2 : 0)>{});
        if constexpr (j > 0 && k >= 2 && IC(t) >= 3 && tt < 5) { GAP(); lds_st<f32x4>(sw[j > 0 ? j - 1 : 0] + (tt < 5 ? tt : 0) * K1, bacc[j > 0 ? j - 1 : 0][tt < 5 ? tt : 0]); GAP(); }
      });
    });
    ST(13);
    waitv<14>();
    BAR();
    if (b == 0 && threadIdx.x == 0) st4(tag, rws, 0, WS_EPOCH);
    f32x4 pr[3][5];
    for (int j = 0; j < 3; ++j) for (int t = 0; t < 5; ++t) pr[j][t] = lds_ld<f32x4>(sr + (j * 5 + t) * K1);
    mfma_done();
    for (int t = 0; t < 5; ++t) {
      const f32x4 s = (bacc[3][t] + pr[0][t]) + (pr[1][t] + pr[2][t]);
      hp[2 * t] = cvt2(lo2(s)); hp[2 * t + 1] = cvt2(hi2(s));
    }
    const u32 so = WS_H + (4 * p + 2 * half + (w >> 1)) * 40960 + 5 * n80 * 1024 + (w & 1) * 256;
    st16(v4u{hp[0], hp[1], hp[2], hp[3]}, rws, gm * 512 + c * 16, so);
    st16(v4u{hp[4], hp[5], hp[6], hp[7]}, rws, gm * 512 + c * 16, so + 2048);
    st8(v2u{hp[8], hp[9]}, rws, (gm >> 1) * 512 + c * 16 + (gm & 1) * 8, so + 4096);
    asm volatile("" ::: "memory");
    float s1 = 0.f, s2 = 0.f;
    for (int i = 0; i < 10; ++i) { s1 = dot2(hp[i], 0x3F803F80u, s1); s2 = dot2(hp[i], hp[i], s2); }
    {
      u32 a = fu(s1), a2 = a, d = fu(s2), d2 = d;
      PIN(a2); PIN(d2);
      auto r1 = __builtin_amdgcn_permlane16_swap(a, a2, false, false);
      auto r2 = __builtin_amdgcn_permlane16_swap(d, d2, false, false);
      a = fu(uf(r1[0]) + uf(r1[1])); d = fu(uf(r2[0]) + uf(r2[1])); a2 = a; d2 = d;
      PIN(a2); PIN(d2);
      auto r3 = __builtin_amdgcn_permlane32_swap(a, a2, false, false);
      auto r4 = __builtin_amdgcn_permlane32_swap(d, d2, false, false);
      s1 = uf(r3[0]) + uf(r3[1]); s2 = uf(r4[0]) + uf(r4[1]);
    }
    st8(v2u{fu(s1), fu(s2)}, rws, gm == 0 ? c * 8 : OOB, WS_HST + (p * 8 + n80) * 1024 + (64 * half + 16 * w) * 8);
  }
  ST(23);
  dma_lin<7>(l16, rws, wcb + (8 + w) * 7168, L0 + (8 + w) * 7168);
  csv = 0.f;
  for (int i = 0; i < 16; ++i) csv += lds_ld<float>(L0 + 84 * K1 + i * 1024 + threadIdx.x * 4);
  PIN(csv);
  waitv<7>(); BAR();
  ST(14);
  if (w == 0 && lane == 0) st4(tag, rws, 0, myflag + 3 * 32768);
  {
    const u32 hl = cvtpk(csv, 0.f) & 0xffffu, pk = hl | (cvtpk(csv - bflo(hl), 0.f) << 16);
    lds_st<v4u>(L0 + 140 * K1 + threadIdx.x * 16, v4u{pk, pk, 0u, 0u});
  }
  sfor<CG0 - 3>([&](auto j) { dma_lin<7>(l16, rws, wcb + (12 + 4 * IC(j) + w) * 7168, L0 + (12 + 4 * IC(j) + w) * 7168); });
  ST(31);
  poll(rws, ((c & 3) + 8 * ((c >> 2) & 1)) * 128, WS_FLG + 3 * 32768 + p * 2048 + (w >> 1) * 512, tag);
  ST(15);
  u32 pvo = (c & 3) * 128;
  {
    auto hload = [&](auto i) { ld_a<h_reg(IC(i)), (IC(i) & 3) * 1024>(l16, rws, WS_H + (4 * p + w) * 40960 + (IC(i) >> 2) * 4096); };
    auto refill = [&](auto i) {
      constexpr int G = CG0 + IC(i) / 7, q = IC(i) % 7;
      if constexpr (G < 10) dma_p<q>(l16, rws, wcb + (4 * G + w) * 7168, L0 + (4 * (G % 5) + w) * 7168);
      else dma_p<q>(l16, rws, wdb + (G - 10) * 7168, L0 + (4 * (G - 10) + w) * 7168);
    };
    sfor<2>([&](auto i) { dma<1, true>(fvo, rws, WS_HST + (p * 8 + 4 * IC(i)) * 1024 + w * 256, L0 + 144 * K1 + w * 2048 + IC(i) * 1024); });
    sfor<C_UP>(hload);
    const u32 tok = lane & 31, hh = lane >> 5;
    u32 avo = (tok >> 4) * 57344 + hh * 256 + (tok & 15) * 16, csa = L0 + 140 * K1 + tok * 16, hsa = L0 + 144 * K1 + w * 2048 + tok * 8;
    PIN(avo); PIN(csa); PIN(hsa); PIN(pvo);
    const u32 aso = WS_ACT + (8 * p + 2 * w) * 57344 + 7 * g * 512;
    auto tso = [&](u32 u) -> u32 { return aso + ((g & 1) ? 6 - u : u) * 512; };
    f32x16 acc[7];
    v4u wf[8], csf[4], mfr, apk;
    f32x2 ssum;
    float var, ke, ee, pr_, dd, av[2];
    for (int i = 0; i < 7; ++i) wf[i] = lds_ld<v4u>(LA(i * 1024));
    GAP();
    auto cstep = [&](auto f_) {
      constexpr int f = IC(f_), ks = f < 160 ? f / 4 : f < 240 ? (f - 160) / 2 : f - 240, u = f < 160 ? f % 4 : f < 240 ? 4 + f % 2 : 6;
      if constexpr (f > 0 && f % 28 == 0) {
        constexpr int j = f / 28 - 1;
        if constexpr (j + 2 >= CG0 && j + 2 < 10) waitv<CP.before[f] - CP.rlast[j + 2] - 1>();
        if constexpr (f == 252) waitv<CP.before[252] - CP.before[240]>();
        RBAR();
        if constexpr (f == 252) { if (w == 0 && lane == 0) st4(tag, rws, 0, myflag + 6 * 32768); }
      }
      if constexpr (f < 160 && f % 4 == 0) waitv<CP.before[f] - CP.hpos[ks] - 1>();
      if constexpr (f + 7 < 280) wf[(f + 7) & 7] = lds_ld<v4u>(LA(((f + 7) % 140) * 1024));
      m32_va<h_reg(ks), ks == 0>(acc[u], wf[f & 7]);
      if constexpr (CP.kind[f] == 1) hload(ic<CP.idx[f]>{});
      if constexpr (CP.kind[f] == 2) refill(ic<CP.idx[f]>{});
      if constexpr (CP.kind[f] == 3) wdreg(ic<CP.idx[f]>{});
      if constexpr (CP.kind[f] == 5) ld_a<4 * CP.idx[f], ((CP.idx[f] + 3) & 3) * 1024>(l16, rws, wdb + ((CP.idx[f] + 35) >> 2) * 4096);
      if constexpr (f == 40) ssum = lds_ld<f32x2>(hsa);
      if constexpr (f > 40 && f <= 54 && f % 2 == 0) { ssum += lds_ld<f32x2>(hsa + ((f - 40) / 2) * 256); PIN(ssum); }
      if constexpr (f == 60) {
        const float mean = ssum[0] * (1.f / 640.f);
        var = fmaxf(ssum[1] * (1.f / 640.f) - mean * mean, 0.f) + 1e-5f;
        ke = __builtin_amdgcn_rsqf(var) * -LOG2E;
        const float nm = hh ? 0.f : -mean;
        const u32 h2 = cvtpk(nm, nm), l2 = cvtpk(nm - bflo(h2), nm - bflo(h2));
        mfr = v4u{h2, l2, 0u, 0u};
        PIN(var); PIN(ke); PIN(mfr);
      }
      if constexpr (f >= 150 && f < 158 && f % 2 == 0) csf[(f - 150) / 2] = lds_ld<v4u>(csa + ((f - 150) / 2) * 512);
      if constexpr (f == 232 || f == 234) csf[(f - 232) / 2] = lds_ld<v4u>(csa + (4 + (f - 232) / 2) * 512);
      if constexpr (f == 272) csf[0] = lds_ld<v4u>(csa + 6 * 512);
      if constexpr (f == C_P1) flag_load<44>(rws, pvo, WS_FLG + 6 * 32768 + p * 2048 + w * 512);
      if constexpr (f == 159) { GAP(); for (int i = 0; i < 4; ++i) m32_vv(acc[i], csf[i], mfr); ST(30); }
      if constexpr (f == 239) { GAP(); for (int i = 0; i < 2; ++i) m32_vv(acc[4 + i], csf[i], mfr); }
      if constexpr (f == 279) { GAP(); m32_vv(acc[6], csf[0], mfr); }
      if constexpr (f >= 160) {
        constexpr int e = (f - 160) / 20, s = (f - 160) % 20, r = (s - 2) / 2;
        if constexpr (s >= 2 && s < 18 && s % 2 == 0) {
          if constexpr (r > 0) asm volatile("v_mul_f32 %0, %1, %2" : "=v"(av[(r - 1) & 1]) : "v"(pr_), "v"(dd));
          asm volatile("v_mul_f32 %0, %2, %3\n\tv_mul_f32 %1, %2, %4\n\tv_exp_f32 %0, %0" : "=&v"(ee), "=&v"(pr_) : "v"(acc[e][r]), "v"(ke), "v"(acc[e][8 + r]));
        }
        if constexpr (s >= 3 && s < 18 && s % 2 == 1) {
          asm volatile("v_fma_f32 %0, %1, %2, %2\n\tv_rcp_f32 %0, %0" : "=&v"(dd) : "v"(ee), "v"(var));
          if constexpr (r >= 2 && r % 2 == 0) { u32 t; asm volatile("v_cvt_pk_bf16_f32 %0, %1, %2" : "=v"(t) : "v"(av[0]), "v"(av[1])); apk[r / 2 - 1] = t; }
        }
        if constexpr (s == 18) { u32 t; asm volatile("v_mul_f32 %1, %2, %3\n\tv_cvt_pk_bf16_f32 %0, %4, %1" : "=v"(t), "=&v"(av[1]) : "v"(pr_), "v"(dd), "v"(av[0])); apk[3] = t; }
        if constexpr (s == 19) st16(apk, rws, avo, tso(e));
      }
      GAP();
    };
    sfor_o<0, 160>(cstep); sfor_o<160, 120>(cstep);
    mfma_done();
    ST(16);
    {
      u32 o[4];
      for (int i = 0; i < 4; ++i) {
        const f32x2 gv = {acc[6][2 * i], acc[6][2 * i + 1]}, uv = {acc[6][8 + 2 * i], acc[6][9 + 2 * i]}, t = gv * ke;
        const f32x2 d = fma2(f32x2{__builtin_amdgcn_exp2f(t[0]), __builtin_amdgcn_exp2f(t[1])}, bc2(var), bc2(var));
        o[i] = cvt2((gv * uv) * f32x2{__builtin_amdgcn_rcpf(d[0]), __builtin_amdgcn_rcpf(d[1])});
      }
      st16(v4u{o[0], o[1], o[2], o[3]}, rws, avo, tso(6));
    }
    ST(17);
  }
  f32x4 acc[4][5];
  u32 wb0 = lb0 + w * 7168, wb1 = wb0 + 57344, wb2 = wb0 + 114688, rd_[3];
  {
    u32 ab[4];
    for (int j = 0; j < 4; ++j) ab[j] = WS_ACT + (8 * p + 4 * half + ((w + 1 + j) & 3)) * 57344 + w * 14336;
    auto aload = [&](auto i) {
      constexpr int k = d_kap(IC(i) < 40 ? IC(i) / 4 : 10 + (IC(i) - 40) % 4), j = IC(i) < 40 ? IC(i) % 4 : (IC(i) - 40) / 4;
      ld_a<act_reg(IC(i)), (k & 3) * 1024>(l16, rws, ab[j] + (k >> 2) * 4096); };
    auto dop = [&](auto o) {
      constexpr int k = DP.kind[IC(o)], i = DP.idx[IC(o)];
      if constexpr (k == 1) aload(ic<i>{});
      if constexpr (k == 2) dma_p<i>(l16, rws, wdb + 4 * 7168, L0 + (16 + w) * 7168);
      if constexpr (k == 4) flag_load<252>(rws, pvo, WS_FLG + 4 * 32768 + p * 2048 + w * 512);
    };
    waitv<CP.before[280] - CP.before[C_P1 + 1] + 1>();
    flag_check<44>(rws, pvo, WS_FLG + 6 * 32768 + p * 2048 + w * 512, tag);
    ST(19);
    sfor<D_UP>(dop);
    for (int t = 0; t < 5; ++t) { acc[3][t] = f32x4{bflo(hp[2 * t]), bfhi(hp[2 * t]), bflo(hp[2 * t + 1]), bfhi(hp[2 * t + 1])}; PIN(acc[3][t]); }
    for (int j = 0; j < 3; ++j) { rd_[j] = lb0 + (4 * j + ((w + 3 - j) & 3)) * 7168; PIN(rd_[j]); }
    PIN(wb0); PIN(wb1); PIN(wb2);
    v4u wf[2][5];
    auto wl = [&](int f) -> u32 { return (f < 14 ? wb0 : f < 28 ? wb1 : wb2) + ((f / 7) & 1) * 28672 + (f % 7) * 1024; };
    waitv<D_UP>();
    for (int t = 0; t < 5; ++t) wf[0][t] = lds_ld<v4u>(wl(t));
    RBAR();
    ST(18);
    if (w == 0 && lane == 0) st4(tag, rws, 0, myflag + 4 * 32768);
    GAP();
    sfor<14>([&](auto k) {
      constexpr int st = IC(k);
      waitv<DP.before[6 * st] - DP.need[st] - 1>();
      if constexpr (st == DP.chk) flag_check<252>(rws, pvo, WS_FLG + 4 * 32768 + p * 2048 + w * 512, tag);
      auto slot = [&](auto m) {
        if constexpr (IC(m) % 3 == 1 && IC(m) < 18) { constexpr int sl = 6 * st + IC(m) / 3; if constexpr (DP.before[sl + 1] > DP.before[sl]) dop(ic<DP.before[sl]>{}); } };
      if constexpr (st < 10) {
        sfor<5>([&](auto t) {
          if constexpr (st < 6) wf[(st + 1) & 1][IC(t)] = lds_ld<v4u>(wl(5 * (st + 1) + IC(t)));
          sfor<4>([&](auto j) {
            if constexpr (st < 7) m16_va<act_reg(4 * st + IC(j)), (st == 0 && IC(j) < 3)>(acc[IC(j)][IC(t)], wf[st & 1][IC(t)]);
            else m16_aa<wd_reg(5 * st + IC(t)), act_reg(4 * st + IC(j)), false>(acc[IC(j)][IC(t)]);
            slot(ic<4 * IC(t) + IC(j)>{});
          });
          GAP();
        });
      } else {
        constexpr int j = st - 10;
        sfor<4>([&](auto q) {
          sfor<5>([&](auto t) {
            m16_aa<wd_reg(5 * (10 + IC(q)) + IC(t)), act_reg(4 * st + IC(q)), false>(acc[j][IC(t)]);
            slot(ic<5 * IC(q) + IC(t)>{});
            constexpr int tt = 5 * IC(q) + IC(t) - 10;
            if constexpr (j > 0 && tt >= 0 && tt % 2 == 0) { GAP(); lds_st<f32x4>((j == 3 ? wb1 : wb0) + (j == 2 ? 28672 : 0) + (tt / 2) * 1024, acc[j > 0 ? j - 1 : 0][tt >= 0 ? tt / 2 : 0]); GAP(); }
          });
        });
      }
    });
    ST(20);
    BAR();
    f32x4 pr[3][5];
    for (int j = 0; j < 3; ++j) for (int t = 0; t < 5; ++t) pr[j][t] = lds_ld<f32x4>(rd_[j] + t * 1024);
    mfma_done();
    u32 op[10];
    for (int t = 0; t < 5; ++t) {
      const f32x4 s = (acc[3][t] + pr[0][t]) + (pr[1][t] + pr[2][t]);
      op[2 * t] = cvt2(lo2(s)); op[2 * t + 1] = cvt2(hi2(s));
    }
    const rsrc_t ro = mkrs(out_, 2048 * 640 * 2);
    const u32 oo = (128 * p + 64 * half + 16 * w + c) * 1280 + n80 * 160;
    __builtin_amdgcn_raw_buffer_store_b128(v4u{op[0], op[1], op[2], op[3]}, ro, oo + gm * 16, 0, 0);
    __builtin_amdgcn_raw_buffer_store_b128(v4u{op[4], op[5], op[6], op[7]}, ro, oo + 64 + gm * 16, 0, 0);
    __builtin_amdgcn_raw_buffer_store_b64(v2u{op[8], op[9]}, ro, oo + 128 + gm * 8, 0, 0);
  }
  asm volatile(".Lfb_end:");
}

extern "C" void kernel_launch(void* const* d_in, const int* in_sizes, int n_in, void* d_out, int out_size, void* d_ws, size_t ws_size,
                              hipStream_t stream) {
  (void)hipFuncSetAttribute((const void*)fused_block, hipFuncAttributeMaxDynamicSharedMemorySize, LDS_BYTES);
  fused_block<<<256, 256, LDS_BYTES, stream>>>(d_in[0], d_in[1], d_in[2], d_in[4], d_in[5], d_in[6], d_out, (char*)d_ws + ((ws_size - WS_END - 4096) & ~(size_t)2097151));
}
```

```cpp
#include <hip/hip_runtime.h>
#include <utility>

#define DI __device__ __forceinline__
#define LAS __attribute__((address_space(3)))
typedef unsigned u32;
typedef u32 v4u __attribute__((ext_vector_type(4)));
typedef u32 v2u __attribute__((ext_vector_type(2)));
typedef float f32x2 __attribute__((ext_vector_type(2)));
typedef float f32x4 __attribute__((ext_vector_type(4)));
typedef float f32x16 __attribute__((ext_vector_type(16)));
typedef __bf16 bf16x2 __attribute__((ext_vector_type(2)));
typedef __amdgpu_buffer_rsrc_t rsrc_t;
#define ACLOB "a0","a1","a2","a3","a4","a5","a6","a7","a8","a9","a10","a11","a12","a13","a14","a15","a16","a17","a18","a19","a20","a21","a22","a23","a24","a25","a26","a27","a28","a29","a30","a31","a32","a33","a34","a35","a36","a37","a38","a39","a40","a41","a42","a43","a44","a45","a46","a47","a48","a49","a50","a51","a52","a53","a54","a55","a56","a57","a58","a59","a60","a61","a62","a63","a64","a65","a66","a67","a68","a69","a70","a71","a72","a73","a74","a75","a76","a77","a78","a79","a80","a81","a82","a83","a84","a85","a86","a87","a88","a89","a90","a91","a92","a93","a94","a95","a96","a97","a98","a99","a100","a101","a102","a103","a104","a105","a106","a107","a108","a109","a110","a111","a112","a113","a114","a115","a116","a117","a118","a119","a120","a121","a122","a123","a124","a125","a126","a127","a128","a129","a130","a131","a132","a133","a134","a135","a136","a137","a138","a139","a140","a141","a142","a143","a144","a145","a146","a147","a148","a149","a150","a151","a152","a153","a154","a155","a156","a157","a158","a159","a160","a161","a162","a163","a164","a165","a166","a167","a168","a169","a170","a171","a172","a173","a174","a175","a176","a177","a178","a179","a180","a181","a182","a183","a184","a185","a186","a187","a188","a189","a190","a191","a192","a193","a194","a195","a196","a197","a198","a199","a200","a201","a202","a203","a204","a205","a206","a207","a208","a209","a210","a211","a212","a213","a214","a215","a216","a217","a218","a219","a220","a221","a222","a223","a224","a225","a226","a227","a228","a229","a230","a231","a232","a233","a234","a235","a236","a237","a238","a239","a240","a241","a242","a243","a244","a245","a246","a247","a248","a249","a250","a251","a252","a253","a254","a255"

constexpr u32 WS_EPOCH = 0;
constexpr u32 WS_FLG = 4096;
constexpr u32 WS_WA = WS_FLG + 8 * 32768;
constexpr u32 WA_G = 153600;
constexpr u32 WS_CSA = WS_WA + 16 * WA_G + 4096;
constexpr u32 WS_WB = WS_CSA + 16 * 16 * 512;
constexpr u32 WS_WC = WS_WB + 8 * 102400;
constexpr u32 WC_G = 286720;
constexpr u32 WS_CSC = WS_WC + 16 * WC_G;
constexpr u32 WS_WD = WS_CSC + 16 * 16 * 1024;
constexpr u32 WS_KVP = WS_WD + 8 * 286720;
constexpr u32 WS_O = WS_KVP + 256 * 3456;
constexpr u32 WS_H = WS_O + 128 * 20480;
constexpr u32 WS_HST = WS_H + 64 * 40960;
constexpr u32 WS_ACT = WS_HST + 128 * 1024;
constexpr u32 WS_END = WS_ACT + 128 * 57344;
#define WC_OFF(g, ks, t) (WS_WC + (size_t)(g) * WC_G + wc_frag(ks, ((g) & 1) ? 6 - (t) : (t)) * 1024)
constexpr int d_kap(int s) { constexpr int o[14] = {0, 6, 7, 13, 1, 5, 8, 12, 2, 4, 9, 11, 3, 10}; return o[s]; }
constexpr unsigned long long d_inv() { unsigned long long v = 0; for (int s = 0; s < 14; ++s) v |= (unsigned long long)s << (4 * d_kap(s)); return v; }
#define WD_OFF(n, ka) (WS_WD + (size_t)(n) * 286720 + ((ka) / 14) * 71680 + ((d_inv() >> (4 * ((ka) % 14))) & 15) * 5120)
constexpr int wd_reg(int f) { return f < 46 ? 4 * (f - 35) : 60 + 4 * (f - 46); }
constexpr int wo_reg(int f) { return f < 23 ? 160 + 4 * f : f == 23 ? 156 : 252; }
constexpr int h_reg(int i) { return i < 15 ? 4 * i : 156 + 4 * (i - 15); }
constexpr int act_reg(int i) { return h_reg(i % 40); }
constexpr int wc_frag(int ks, int u) { return u < 3 ? 3 * ks + u : u < 5 ? 117 + 2 * ks + u : 40 * u + ks; }
#define ST(k)
constexpr int LDS_BYTES = 163840;
constexpr u32 K1 = 1024;
constexpr float LOG2E = 1.4426950408889634f;

template <class F, int... Is> DI void sfor_(F&& f, std::integer_sequence<int, Is...>) { (f(std::integral_constant<int, Is>{}), ...); }
template <int N, class F> DI void sfor(F&& f) { sfor_(f, std::make_integer_sequence<int, N>{}); }
template <int O, class F, int... Is> DI void sfor_o_(F&& f, std::integer_sequence<int, Is...>) { (f(std::integral_constant<int, O + Is>{}), ...); }
template <int O, int N, class F> DI void sfor_o(F&& f) { sfor_o_<O>(f, std::make_integer_sequence<int, N>{}); }
#define IC(x) decltype(x)::value
template <int N> using ic = std::integral_constant<int, N>;

#define GAP() __builtin_amdgcn_sched_barrier(0)
#define BAR() asm volatile("s_waitcnt lgkmcnt(0)\n\ts_barrier" ::: "memory", ACLOB)
#define RBAR() asm volatile("s_barrier" ::: "memory")
template <class T> DI void pin_(T& x) { asm volatile("" : "+v"(x)); }
#define PIN(x) pin_(x)
template <int N> DI void waitv() { static_assert(N >= 0, "bad count"); asm volatile("s_waitcnt vmcnt(%c0)" ::"i"(N > 63 ? 63 : N) : "memory"); }
DI void waitl() { asm volatile("s_waitcnt lgkmcnt(0)" ::: "memory"); }
DI void mfma_done() { GAP(); asm volatile("s_nop 11" ::: "memory"); GAP(); }

template <int A, int IMM> DI void ld_a(u32 voff, rsrc_t rs, u32 soff) {
  asm volatile("buffer_load_dwordx4 a[%c3:%c4], %0, %1, %2 offen offset:%c5 sc1" ::"v"(voff), "s"(rs), "s"(soff), "i"(A), "i"(A + 3), "i"(IMM) : "memory");
}
DI void ld_v(v4u& d, u32 voff, rsrc_t rs, u32 soff) { asm volatile("buffer_load_dwordx4 %0, %1, %2, %3 offen sc1" : "=v"(d) : "v"(voff), "s"(rs), "s"(soff) : "memory"); }
template <int N, int SC1> DI void dma(u32 voff, rsrc_t rs, u32 soff, u32 ldsa) {
#define DMA1(o, s) "\n\tbuffer_load_dwordx4 %0, %1, %2 offen offset:" #o s " lds"
#define DMAH "s_mov_b32 m0, %3\n\ts_nop 0"
#define DMAN(s) \
  if constexpr (N == 1) asm volatile(DMAH DMA1(0, s)::"v"(voff), "s"(rs), "s"(soff), "s"(ldsa) : "memory"); \
  if constexpr (N == 2) asm volatile(DMAH DMA1(0, s) DMA1(1024, s)::"v"(voff), "s"(rs), "s"(soff), "s"(ldsa) : "memory"); \
  if constexpr (N == 3) asm volatile(DMAH DMA1(0, s) DMA1(1024, s) DMA1(2048, s)::"v"(voff), "s"(rs), "s"(soff), "s"(ldsa) : "memory"); \
  if constexpr (N == 4) asm volatile(DMAH DMA1(0, s) DMA1(1024, s) DMA1(2048, s) DMA1(3072, s)::"v"(voff), "s"(rs), "s"(soff), "s"(ldsa) : "memory");
  if constexpr (SC1 == 1) { DMAN(" sc1") } else if constexpr (SC1 == 0) { DMAN(" nt") } else { DMAN("") }
}
template <int N> DI void dma_lin(u32 voff, rsrc_t rs, u32 soff, u32 ldsa) {
  sfor<(N + 3) / 4>([&](auto i) { constexpr int r = (N - 4 * IC(i)) < 4 ? (N - 4 * IC(i)) : 4; dma<r, true>(voff, rs, soff + 4096u * IC(i), ldsa + 4096u * IC(i)); });
}
template <int I> DI void dma_p(u32 voff, rsrc_t rs, u32 soff, u32 ldsa) {
  if constexpr ((I & 3) == 0) dma<1, true>(voff, rs, soff + 1024u * I, ldsa + 1024u * I);
  else asm volatile("buffer_load_dwordx4 %0, %1, %2 offen offset:%c3 sc1 lds" ::"v"(voff), "s"(rs), "s"(soff + 1024u * (I & ~3)), "i"(1024 * (I & 3)) : "memory");
}
DI void set_m0(u32 ldsa) { asm volatile("s_mov_b32 m0, %0\n\ts_nop 0" ::"s"(ldsa) : "memory"); }
template <int I, bool NT> DI void dma_i(u32 voff, rsrc_t rs, u32 soff) {
  if constexpr (NT) asm volatile("buffer_load_dwordx4 %0, %1, %2 offen offset:%c3 nt lds" ::"v"(voff), "s"(rs), "s"(soff), "i"(1024 * I) : "memory");
  else asm volatile("buffer_load_dwordx4 %0, %1, %2 offen offset:%c3 lds" ::"v"(voff), "s"(rs), "s"(soff), "i"(1024 * I) : "memory");
}
template <int A, int IMM> DI void ds_a(u32 addr) {
  asm volatile("ds_read_b128 a[%c1:%c2], %0 offset:%c3" ::"v"(addr), "i"(A), "i"(A + 3), "i"(IMM) : "memory");
}
template <int IMM> DI void trr(v2u& r, u32 addr) { asm volatile("ds_read_b64_tr_b16 %0, %1 offset:%c2" : "=v"(r) : "v"(addr), "i"(IMM) : "memory"); }
DI void tr_wait8(v2u (&r)[8]) {
  asm volatile("s_waitcnt lgkmcnt(0)" : "+v"(r[0]), "+v"(r[1]), "+v"(r[2]), "+v"(r[3]), "+v"(r[4]), "+v"(r[5]), "+v"(r[6]), "+v"(r[7])::"memory");
}
DI v4u cat(v2u a, v2u b) { return v4u{a.x, a.y, b.x, b.y}; }
#define M16 "v_mfma_f32_16x16x32_bf16 "
#define M32 "v_mfma_f32_32x32x16_bf16 "
template <int A, bool Z> DI void m16_av(f32x4& c, const v4u& b) {
  if constexpr (Z) asm volatile(M16 "%0, a[%c2:%c3], %1, 0" : "=&v"(c) : "v"(b), "i"(A), "i"(A + 3));
  else asm volatile(M16 "%0, a[%c2:%c3], %1, %0" : "+v"(c) : "v"(b), "i"(A), "i"(A + 3));
}
template <int B, bool Z> DI void m16_va(f32x4& c, const v4u& a) {
  if constexpr (Z) asm volatile(M16 "%0, %1, a[%c2:%c3], 0" : "=&v"(c) : "v"(a), "i"(B), "i"(B + 3));
  else asm volatile(M16 "%0, %1, a[%c2:%c3], %0" : "+v"(c) : "v"(a), "i"(B), "i"(B + 3));
}
template <int A, int B, bool Z> DI void m16_aa(f32x4& c) {
  if constexpr (Z) asm volatile(M16 "%0, a[%c1:%c2], a[%c3:%c4], 0" : "=&v"(c) : "i"(A), "i"(A + 3), "i"(B), "i"(B + 3));
  else asm volatile(M16 "%0, a[%c1:%c2], a[%c3:%c4], %0" : "+v"(c) : "i"(A), "i"(A + 3), "i"(B), "i"(B + 3));
}
template <bool Z> DI void m16_vv(f32x4& c, const v4u& a, const v4u& b) {
  if constexpr (Z) asm volatile("s_nop 1\n\t" M16 "%0, %1, %2, 0" : "=&v"(c) : "v"(a), "v"(b));
  else asm volatile("s_nop 1\n\t" M16 "%0, %1, %2, %0" : "+v"(c) : "v"(a), "v"(b));
}
template <int B, bool Z> DI void m32_va(f32x16& c, const v4u& a) {
  if constexpr (Z) asm volatile(M32 "%0, %1, a[%c2:%c3], 0" : "=&v"(c) : "v"(a), "i"(B), "i"(B + 3));
  else asm volatile(M32 "%0, %1, a[%c2:%c3], %0" : "+v"(c) : "v"(a), "i"(B), "i"(B + 3));
}
DI void m32_vv(f32x16& c, const v4u& a, const v4u& b) { asm volatile(M32 "%0, %1, %2, %0" : "+v"(c) : "v"(a), "v"(b)); }
DI u32 cvt2(f32x2 v) { return __builtin_bit_cast(u32, __builtin_convertvector(v, bf16x2)); }
DI u32 cvtpk(float lo, float hi) { return cvt2(f32x2{lo, hi}); }
DI float bflo(u32 u) { return __builtin_bit_cast(float, u << 16); }
DI float bfhi(u32 u) { return __builtin_bit_cast(float, u & 0xffff0000u); }
DI u32 fu(float f) { return __builtin_bit_cast(u32, f); }
DI float uf(u32 u) { return __builtin_bit_cast(float, u); }
DI f32x2 lo2(f32x4 v) { return __builtin_shufflevector(v, v, 0, 1); }
DI f32x2 hi2(f32x4 v) { return __builtin_shufflevector(v, v, 2, 3); }
DI f32x2 fma2(f32x2 a, f32x2 b, f32x2 c) { return __builtin_elementwise_fma(a, b, c); }
DI f32x2 bc2(float a) { return f32x2{a, a}; }
DI float dot2(u32 a, u32 b, float c) { return __builtin_amdgcn_fdot2_f32_bf16(__builtin_bit_cast(bf16x2, a), __builtin_bit_cast(bf16x2, b), c, false); }
DI float sum4(v2u v, float c) { return dot2(v.y, 0x3F803F80u, dot2(v.x, 0x3F803F80u, c)); }
DI float exp_le1(float x) { return __builtin_amdgcn_fmed3f(__builtin_amdgcn_exp2f(x), 0.f, 1.f); }
DI f32x2 phi2(f32x2 te) {
  const f32x2 a = te * LOG2E;
  return f32x2{fmaxf(te[0], 1e-6f), fmaxf(te[1], 1e-6f)} + f32x2{exp_le1(a[0]), exp_le1(a[1])};
}
template <class T> DI T lds_ld(u32 a) { return *(const LAS T*)(size_t)a; }
template <class T> DI void lds_st(u32 a, T v) { *(LAS T*)(size_t)a = v; }
DI void st16(v4u v, rsrc_t rs, u32 voff, u32 soff) {
  asm volatile("buffer_store_dwordx4 %0, %1, %2, %3 offen sc1\n\ts_nop 1" ::"v"(v), "v"(voff), "s"(rs), "s"(soff) : "memory");
}
DI void st8(v2u v, rsrc_t rs, u32 voff, u32 soff) { __builtin_amdgcn_raw_buffer_store_b64(v, rs, voff, soff, 16); }
DI void st4(u32 v, rsrc_t rs, u32 voff, u32 soff) { __builtin_amdgcn_raw_buffer_store_b32(v, rs, voff, soff, 16); }
DI rsrc_t mkrs(const void* p, u32 bytes) { return __builtin_amdgcn_make_buffer_rsrc((void*)p, 0, bytes, 0x00020000); }
constexpr u32 OOB = 0x7ff00000u;

DI void poll(rsrc_t rs, u32 voff, u32 soff, u32 tag) {
#pragma unroll 1
  for (u32 spins = 0; spins < (1u << 16); ++spins) {
    const u32 v = __builtin_amdgcn_raw_buffer_load_b32(rs, voff, soff, 16);
    if (__all(v == tag)) break;
    asm volatile("" ::: "memory");
  }
  asm volatile("" ::: "memory");
}

template <int A> DI void flag_check(rsrc_t rs, u32 voff, u32 soff, u32 tag) {
  u32 fv;
  asm volatile("v_accvgpr_read_b32 %0, a%c1" : "=v"(fv) : "i"(A) : "memory");
  if (!__all(fv == tag)) poll(rs, voff, soff, tag);
}
DI void flag_ldv(u32& d, rsrc_t rs, u32 voff, u32 soff) { asm volatile("buffer_load_dword %0, %1, %2, %3 offen sc1" : "=v"(d) : "v"(voff), "s"(rs), "s"(soff) : "memory"); }
DI void flag_ckv(u32& d, rsrc_t rs, u32 voff, u32 soff, u32 tag) {
  asm volatile("; LANDED" : "+v"(d)::"memory");
  if (!__all(d == tag)) poll(rs, voff, soff, tag);
}
template <int A> DI void flag_load(rsrc_t rs, u32 voff, u32 soff) {
  asm volatile("buffer_load_dword a%c3, %0, %1, %2 offen sc1" ::"v"(voff), "s"(rs), "s"(soff), "i"(A) : "memory");
}

#define MAPV 0
#define KNOBS
#define XNTV 2
#define XGV 7
#define QF1V 4
#define QKVV 16
#define RAWV 0
constexpr int XNT = XNTV;
constexpr int X_G = XGV;
constexpr int Q_F1 = QF1V, Q_KV = QKVV;
#define ST0V 15
#define STSV 2
#define QS0V 35
#define XPREV 1
#define RPREV 2
#define XMIDV 2
#define XFV 1
#define QWOV 18
#define QSSV 4
#define QSNV 9
#define XPOL " nt"
#define XTV 3
constexpr int X_T = XTV, X_F = XFV, Q_WO = QWOV, Q_SS = QSSV, Q_SN = QSNV;
constexpr int QS0 = QS0V, X_PRE = XPREV, R_PRE = RPREV, X_MID = XMIDV;
constexpr int ST0 = ST0V, STS = STSV;

#define PSITES 10, 18, 8, 8, 12, 8, 18, 8, 8
constexpr int PS_[] = {PSITES};
constexpr int N_E = PS_[0], NV = PS_[1], P_C = PS_[2], P_D = P_C + PS_[3], P_E = P_D + PS_[4], P_F = P_E + PS_[5], P_G = P_F + PS_[6], P_H = P_G + PS_[7], P_T = PS_[8];
static_assert(P_H <= 51 + NV, "list too short");
#define APAT "11111201111120111112332033230332033232330323030000"
constexpr char A_PAT[] = APAT;
struct APlan { int kind[50], idx[50], before[51], glast[5], nwo, ok; };
constexpr APlan make_aplan() {
  APlan P{};
  int n = 0, ng = 10, ns = 0, nq = 0, nw = 0;
  for (int s = 0; s < 50; ++s) {
    P.before[s] = n;
    const int k = A_PAT[s] - '0';
    P.kind[s] = k;
    if (k == 1) { P.idx[s] = ng; if (ng % 5 == 4) P.glast[ng / 5] = n; ++ng; }
    if (k == 2) P.idx[s] = ns++;
    if (k == 3) P.idx[s] = nq++;
    if (k == 4) P.idx[s] = nw++;
    if (k && !(k == 3 && nq > 10)) ++n;
  }
  P.before[50] = n; P.nwo = nw;
  P.ok = ng == 25 && ns == 9 && nq == 15;
  return P;
}
constexpr APlan AP = make_aplan();
static_assert(AP.ok, "plan incomplete");

static_assert(NV == 18, "down-weight fragments 28..45 live in VGPRs");
#define CF1V 276
#define CNA 12
constexpr int C_UP = 8, CG0 = 5, C_E0 = 124, C_F0 = 222, C_CK = 240, C_F1 = CF1V, C_NA = CNA;
struct CPlan { int kind[280], idx[280], before[281], hpos[40], rlast[16], fpos[2], apos[16], ok; };
constexpr CPlan make_cplan() {
  CPlan P{};
  int nh = C_UP, nr = 0, na = 0, nf = 0, n = 2 + C_UP;
  for (int i = 0; i < C_UP; ++i) P.hpos[i] = 2 + i;
  for (int f = 0; f < 280; ++f) {
    const int avail = 7 * (f / 28 + 5 - CG0), s = (f - C_E0) % 26;
    P.before[f] = n;
    int k = 0;
    const bool slot = f < C_E0 ? (f & 1) : (s % 3 == 2 || (f >= 240 && s % 3 == 1 && s < 24));
    if (f >= C_E0 && s == 25) k = 4;
    else if (slot) {
      if (nh < 40 && nh < f / 3 + 10) k = 1;
      else if ((f >= C_F0 && nf == 0) || (f >= C_F1 && nf == 1)) k = 6;
      else if (nr < avail) k = 2;
      else if (nh < 40) k = 1;
      else if (na < C_NA && f > C_CK + na) k = 5;
    }
    P.kind[f] = k;
    if (k == 1) { P.idx[f] = nh; P.hpos[nh++] = n; }
    if (k == 2) { P.idx[f] = nr; if (nr % 7 == 6) P.rlast[CG0 + nr / 7] = n; ++nr; }
    if (k == 5) { P.idx[f] = na; P.apos[na++] = n; }
    if (k == 6) { P.idx[f] = nf; P.fpos[nf++] = n; }
    if (k) ++n;
  }
  P.before[280] = n;
  P.ok = (nr == 7 * (14 - CG0) && nh == 40 && na == C_NA && nf == 2);
  return P;
}
constexpr CPlan CP = make_cplan();
static_assert(CP.ok, "plan incomplete");

#define DPAR 0, 2, 6, 24, 1
constexpr int DP_[] = {DPAR};
constexpr int D_UP = DP_[0], D_PUB = DP_[1], D_CHK = DP_[2], D_LEAD = DP_[3], D_CK1 = DP_[4];
struct DPlan { int kind[96], idx[96], before[85], need[14], n0, ok; };
constexpr DPlan make_dplan() {
  DPlan P{};
  P.n0 = CP.before[280] + 1;
  int n = 0, na = C_NA, n4 = 0, apos[56] = {}, fpos = 0;
  for (int i = 0; i < C_NA; ++i) apos[i] = CP.apos[i] - P.n0;
  for (int s = -D_UP; s < 84; ++s) {
    const int st = s < 0 ? -1 : s / 6;
    if (s >= 0) P.before[s] = n;
    int k = 0;
    if (st == D_CHK - 2 && !n4) k = 4;
    else if (na < 56 && na < D_LEAD + 4 * (st + 1) && (na < 16 || st >= D_CK1) && (na < 32 || st >= D_CHK)) k = 1;
    if (!k) continue;
    P.kind[n] = k;
    if (k == 1) { P.idx[n] = na; apos[na++] = n; }
    if (k == 4) { n4 = 1; fpos = n; }
    ++n;
  }
  P.before[84] = n;
  for (int st = 0; st < 14; ++st) {
    int nd = apos[4 * st + 3];
    if (st == 3 && CP.rlast[13] - P.n0 > nd) nd = CP.rlast[13] - P.n0;
    if (st == D_PUB && nd < -1) nd = -1;
    if (st == D_CK1 && CP.fpos[1] - P.n0 > nd) nd = CP.fpos[1] - P.n0;
    if (st == D_CHK && fpos > nd) nd = fpos;
    P.need[st] = nd;
  }
  P.ok = na == 56 && n4 == 1;
  for (int st = 0; st < 14; ++st) if (P.need[st] >= P.before[6 * st]) P.ok = 0;
  return P;
}
constexpr DPlan DP = make_dplan();
static_assert(DP.ok, "plan incomplete");

__global__ __launch_bounds__(256, 1) void fused_block(const void* __restrict__ x_, const void* __restrict__ wqkv_, const void* __restrict__ wo_,
                                                      const void* __restrict__ wg_, const void* __restrict__ wu_, const void* __restrict__ wd_,
                                                      void* __restrict__ out_, void* ws_) {
  extern __shared__ __attribute__((aligned(16))) char lds_[];
  const u32 L0 = (u32)(size_t)(LAS char*)lds_;
  ST(0);
  const u32 b = blockIdx.x, xcd = b & 7, r_ = b >> 3;
  const u32 p = (xcd >> 1) * 4 + (r_ >> 3), ch = xcd & 1, cc = r_ & 7, g = 8 * ch + cc,
            n80 = 4 * ch + (cc & 3), half = cc >> 2;
  const u32 lane = threadIdx.x & 63, w = __builtin_amdgcn_readfirstlane(threadIdx.x >> 6);
  const u32 c = lane & 15, gm = lane >> 4, l16 = lane * 16;
  asm volatile("s_nop 4" ::: "memory");

  const u32 cr = (c - 4 * gm) & 15;
  u32 rvo2 = gm * 1280 + (cr < 9 ? cr : 9) * 16;
  {
    const u32 c14 = cr < 14 ? cr : 14, sec = (c14 * 13) >> 6, vo = gm * 3840 + sec * 1200 + c14 * 16;
    const rsrc_t rq = mkrs(wqkv_, 640 * 1920 * 2), rw = mkrs(wo_, 640 * 640 * 2);
    const u32 sq = (40 * p + 16 * w) * 3840 + g * 80, sw_ = (40 * p + 12 * half) * 1280 + n80 * 160;
    set_m0(L0 + 4 * w * K1);
    if (w < 2) sfor<4>([&](auto i) { dma_i<IC(i), true>(vo, rq, sq + IC(i) * (4 * 3840 - 1024)); });
    else if (w == 2) {
      dma_i<0, true>(vo, rq, sq); dma_i<1, true>(vo, rq, sq + 4 * 3840 - 1024);
      set_m0(L0 + 10 * K1); dma_i<0, true>(rvo2, rw, sw_); dma_i<1, true>(rvo2, rw, sw_ + 4 * 1280 - 1024);
    } else { dma_i<0, true>(rvo2, rw, sw_ + 8 * 1280); sfor<3>([&](auto i) { dma_i<IC(i) + 1, true>(rvo2, rw, sw_ + 20 * 1280 - 1024 + IC(i) * (4 * 1280 - 1024)); }); }
  }
  const rsrc_t rx = mkrs(x_, 2048 * 640 * 2), rws = mkrs(ws_, WS_END);
  flag_load<247>(rws, 0, WS_EPOCH);
  const u32 rot0 = MAPV == 1 ? ((5 * g) >> 3) + 5 * (r_ >> 4) : ((5 * cc) >> 2) + 3 * (r_ >> 3), rot = rot0 >= 10 ? rot0 - 10 : rot0;
  const u32 xl = L0 + (40 + 16 * w) * K1;
  u32 xv[4];
  xv[0] = (lane >> 3) * 1280 + (((lane & 7) ^ ((lane >> 3) & 6)) * 16);
  for (int i = 1; i < 4; ++i) { xv[i] = xv[0] + i * (10240 - 1024); PIN(xv[i]); }
  const u32 xso = (128 * p + 32 * w) * 1280;
  auto rblk = [&](u32 cb) -> u32 { const u32 v = cb + rot; return v >= 10 ? v - 10 : v; };
  auto xpiece = [&](auto cb, auto i) {
    if constexpr (IC(i) == 0) set_m0(xl + (IC(cb) & 3) * 4 * K1);
    dma_i<IC(i), (IC(cb) < XNT)>(xv[IC(i)], rx, xso + rblk(IC(cb)) * 128); };
  auto xblock = [&](auto cb) { sfor<4>([&](auto i) { xpiece(cb, i); }); };
  xblock(ic<0>{});
  {
    const u32 v = 4 + 5 * half + 3 * (r_ >> 3);
    asm volatile("buffer_load_dwordx4 a[240:243], %0, %1, %2 offen" XPOL ::"v"(xv[0]), "s"(rx), "s"(xso + (v >= 10 ? v - 10 : v) * 128 + (cc & 3) * 10240) : "memory");
  }
  {
    unsigned long long pc; u32 len;
    asm volatile("s_getpc_b64 %0\n.Lfb_pc:\n\ts_mov_b32 %1, .Lfb_end-.Lfb_pc" : "=s"(pc), "=s"(len));
    const rsrc_t rc = mkrs((const void*)(pc & ~255ull), len);
    asm volatile("buffer_load_dwordx2 a[248:249], %0, %1, %2 offen" ::"v"(lane * 8), "s"(rc), "s"((r_ * 4 + w) * 512) : "memory");
  }
  ST(24);
  const u32 myflag = WS_FLG + (p * 16 + g) * 128;
  const u32 q4 = c >> 2, pp = c & 3;
  auto rowb = [&](u32 row, u32 byte) -> u32 { return row * 256 + ((byte + 64 * row) & 255); };
  u32 lb0 = L0 + l16, lb1 = lb0 + 61440u, lb2 = lb0 + 122880u;
  auto LA = [&](u32 off) -> u32 { return (off < 61440u ? lb0 : off < 122880u ? lb1 : lb2) + off % 61440u; };
  u32 fvo = gm * 1024 + c * 16;
  const u32 colb = (32 * (gm >> 1) + 8 * pp + 4 * (gm & 1)) * 2;
  u32 tag, wflag_vo = (c * 16 + 4 * (gm & 1)) * 128, rvo1 = gm * 3456 + (cr < 13 ? cr : 13) * 16;
  const u32 wflag_so = WS_FLG + (8 * ch + (cc & 3)) * 128;
  constexpr u32 KVP_L = 40 * 1024, KVS = 150 * 1024;
  u32 xrv, xra, e0, e1, kvo, kva, qb1, qb2, sb1, sb4, csa_, ckb;
  float ck[10];
  {
    const u32 tl = 2 * w + (gm & 1);
    const u32 c0 = w == 0 ? 40 : w == 1 ? 56 : w == 2 ? 100 : 20, c1 = w == 0 ? 60 : w == 1 ? 80 : w == 2 ? 0 : 16;
    const u32 col = ((gm & 1) ? c1 : c0) + (tl == 2 ? 20 * pp : tl == 7 ? 20 * (pp & 1) : 4 * pp);
    u32 a0 = L0 + (gm >> 1) * 2048 + rowb(q4, col * 2), d[4];
    const u32 oA = (tl == 7 && c >= 8) ? OOB : (tl < 5 ? tl * 1024 : 97280 + tl * 1024) + c * 16, oB = tl < 5 ? 5120 : 2560, oC = tl < 7 ? 256 : 128;
    for (int r = 0; r < 3; ++r) { const u32 B = 5 * p + 2 * r + (gm >> 1); d[r] = oA + (B >> 2) * oB + (B & 3) * oC; }
    d[2] = gm >= 2 ? OOB : d[2];
    const u32 j = w < 3 ? w : (gm < 3 ? gm : 0);
    const bool s4 = half && j == 1;
    const u32 cw = w < 3 ? colb : 128 + 8 * pp, pc1 = L0 + (10 + (s4 ? 1 + 3 * (q4 & 1) : j)) * K1;
    u32 a1 = pc1 + rowb(s4 ? q4 >> 1 : q4, cw), a2 = s4 ? pc1 + rowb(2 + (q4 >> 1), cw) : a1 + 3 * K1;
    d[3] = (w < 3 ? w : gm) >= 3 - half ? OOB : (s4 ? 4 * 5120 + (p & 3) * 256 : (p & 3) * 5120 + (3 * half + j) * 256) + (w < 3 ? fvo : 4096 + c * 16);
    const u32 r = c & 7, rb = xl + (c >> 3) * K1 + r * 128;
    e0 = rb + ((gm ^ (r & 6)) * 16); e1 = e0 ^ 64;
    PIN(a0); PIN(a1); PIN(a2); PIN(e0); PIN(e1);
    for (int i = 0; i < 4; ++i) PIN(d[i]);
    sfor_o<1, X_MID - 1>(xblock);
    ST(2);
    waitv<4 * X_MID + 2>();
    RBAR();
    ST(26);
    v2u r0[8];
    sfor<3>([&](auto r) { trr<4096 * IC(r)>(r0[2 * IC(r)], a0); trr<4096 * IC(r) + 1024>(r0[2 * IC(r) + 1], a0); });
    trr<0>(r0[6], a1); trr<0>(r0[7], a2);
    { u32 e; asm volatile("v_accvgpr_read_b32 %0, a247" : "=v"(e)::"memory"); tag = __builtin_amdgcn_readfirstlane(e) + 1; }
    tr_wait8(r0);
    for (int i = 0; i < 3; ++i) st16(cat(r0[2 * i], r0[2 * i + 1]), rws, d[i], WS_WA + g * WA_G);
    st16(cat(r0[6], r0[7]), rws, d[3], WS_WB + n80 * 102400 + (p >> 2) * 25600);
    float s01 = sum4(r0[3], sum4(r0[2], sum4(r0[1], sum4(r0[0], 0.f)))), s2 = sum4(r0[5], sum4(r0[4], 0.f));
    u32 sa = fu(s01 + (gm < 2 ? s2 : 0.f)), sb = sa;
    PIN(sb);
    auto sw2 = __builtin_amdgcn_permlane32_swap(sa, sb, false, false);
    st4(fu(uf(sw2[0]) + uf(sw2[1])), rws, lane < 32 ? lane * 4 : OOB, WS_CSA + (g * 16 + p) * 512 + w * 128);
    ST(27);
  }
    auto rawop = [&](auto o) {
      constexpr int i = IC(o);
      if constexpr (i == 0 || i == 4) set_m0(L0 + (104 + 5 * w + i) * K1);
      if constexpr (i == 5) set_m0(L0 + (124 + 4 * w) * K1);
      if constexpr (i < 5) dma_i<i % 4, true>(rvo1, mkrs(w < 2 ? wg_ : wu_, 640 * 1728 * 2), (40 * p + 20 * (w & 1)) * 3456 + g * 224 + i * 4 * 3456 - (i % 4) * 1024);
      else dma_i<(i + 3) % 4, true>(rvo2, mkrs(wd_, 1728 * 640 * 2), (64 * (2 * p + half) + 16 * w) * 1280 + n80 * 160 + (i - 5) * (4 * 1280 - 1024));
    };
  sfor<9>(rawop);
  u32 ra[3], ro_[7];
  {
    const u32 t = 2 * w + (gm >> 1), u = (g & 1) ? 6 - t : t;
    ra[0] = L0 + (104 + 10 * (gm & 1)) * K1 + rowb(q4, (t < 7 ? t : 6) * 32 + pp * 8);
    ra[1] = L0 + (124 + 4 * w) * K1 + rowb(q4, colb); ra[2] = L0 + (124 + 4 * w + (gm & 1)) * K1 + rowb(q4, 128 + 8 * pp);
    const u32 A = t < 7 ? (u < 3 ? u : u < 5 ? 117 + u : 40 * u) * 1024 + (gm & 1) * 256 + c * 16 : OOB, Bk = u < 3 ? 3072 : u < 5 ? 2048 : 1024;
    for (int i = 0; i < 5; ++i) ro_[i] = A + ((5 * p + i) >> 1) * Bk;
    ro_[5] = t < 7 ? u * 128 + (lane & 31) * 4 : OOB;
    ro_[6] = gm < 2 ? 4096 + l16 : OOB;
    for (int i = 0; i < 3; ++i) PIN(ra[i]);
    for (int i = 0; i < 7; ++i) PIN(ro_[i]);
  }
  waitv<9>(); RBAR();
  if (w == 0 && lane == 0) st4(tag, rws, 0, myflag);
  ST(28);
  const u32 T = 4 * (2 * p + half) + w;
  u32 wdk = T < 112 ? (u32)WD_OFF(n80, T >> 1) + (w & 1) * 512 : OOB;
  asm volatile("" : "+s"(wdk));
  {
    const u32 tid = threadIdx.x, row = (tid * 205) >> 10, dq = tid - 5 * row, hd = row >= 21, e = row - 21 * hd;
    const u32 tile = e < 16 ? (dq < 4 ? hd : 4 + hd) : (dq < 4 ? 2 + hd : 6), cl = e < 16 ? e : e == 20 ? 0 : e - 8 + 4 * hd;
    kva = L0 + KVP_L + tile * 1024 + ((dq < 4 ? dq : hd) * 16 + (cl & 15)) * 16;
    kvo = tid < 210 ? tid * 16 : OOB; qb1 = lb0 + 100 * K1; qb2 = L0 + 100 * K1 + 2048 + gm * 128 + (c & 7) * 16;
    sb1 = L0 + 150 * K1 + w * 256 + c * 4; sb4 = L0 + 150 * K1 + w * 256 + gm * 16;
    csa_ = L0 + 152 * K1 + (lane >> 5) * 4096 + w * 128 + (lane & 31) * 4; ckb = L0 + 152 * K1 + c * 4;
    { const u32 l60 = lane < 60 ? lane : 59; xrv = (l60 / 10) * 1280 + (l60 % 10) * 16; xra = L0 + (8 + w) * 7168 + (c / 6) * K1 + (c % 6) * 160 + gm * 8; PIN(xrv); PIN(xra); }
    PIN(kva); PIN(kvo); PIN(qb1); PIN(qb2); PIN(sb1); PIN(sb4); PIN(csa_); PIN(ckb); PIN(lb0); PIN(lb1); PIN(lb2); PIN(fvo); PIN(wflag_vo); PIN(rvo1); PIN(rvo2);
  }
  sfor_o<X_MID, 3 - X_MID>(xblock);
  f32x4 rsr[2], nmr[2]; float rsl[2], nml[2];
  v4u ones = {0x3F803F80u, 0x3F803F80u, 0x3F803F80u, 0x3F803F80u};
  PIN(ones);
  const u32 wob = WS_WB + n80 * 102400 + w * 25600;
  auto woload = [&](auto i) { ld_a<wo_reg(IC(i)), (IC(i) & 3) * 1024>(l16, rws, wob + (IC(i) >> 2) * 4096); };
  f32x4 kvacc[2][5];
  {
    const u32 wa = WS_WA + g * WA_G;
    auto rks = [&](u32 ks) -> u32 { const u32 v = ks + 2 * rot; return v >= 20 ? v - 20 : v; };
    auto gop = [&](auto i) { dma_p<IC(i) % 5>(l16, rws, wa + rks(4 * (IC(i) / 5) + w) * 5120, L0 + (4 * (IC(i) / 5) + w) * 5120); };
    f32x4 S[2], G[2];
    v4u rt[8]; float rcs;
    auto xstat1 = [&](auto cb, auto i) { constexpr int mt = IC(i) >> 1, ks = 2 * IC(cb) + (IC(i) & 1);
      m16_av<4 * (20 * mt + ks), ks == 0>(S[mt], ones);
      m16_aa<4 * (20 * mt + ks), 4 * (20 * mt + ks), ks == 0>(G[mt]); };
    auto gpre = [&](auto i) { if constexpr (IC(i) < 10) gop(i); else dma_p<IC(i) - 10>(l16, rws, WS_CSA + g * 8192 + w * 2048, L0 + 152 * K1 + w * 2048); };
    static_assert(X_G >= 7, "all x requests precede the stream requests");
    sfor<10>([&](auto i) {
      constexpr int cb = IC(i), out = cb + 2 < 9 ? cb + 2 : 9;
      waitv<4 * (out - cb) + (cb < X_MID ? 9 : 0) + (cb > X_G - 2 && cb <= X_G + 1 ? 1 : 0) + (cb > X_G ? 12 : 0)>();
      sfor<4>([&](auto i) { constexpr int mt = IC(i) >> 1, ks = 2 * cb + (IC(i) & 1); ds_a<4 * (20 * mt + ks), ((cb & 3) * 4 + 2 * mt) * 1024>((ks & 1) ? e1 : e0); });
      if constexpr (cb > 0) asm volatile("s_waitcnt lgkmcnt(4)" ::: "memory");
      if constexpr (cb == X_G) { ST(1); waitv<4 * (9 - (cb + 1 < 9 ? cb + 1 : 9))>(); flag_check<255>(rws, wflag_vo, wflag_so, tag); ST(3); }
      sfor<4>([&](auto q) {
        if constexpr (cb > 0) xstat1(ic<(cb > 0 ? cb - 1 : 0)>{}, q);
        if constexpr (cb + 3 < 10) xpiece(ic<(cb + 3 < 10 ? cb + 3 : 0)>{}, q);
        if constexpr (cb == X_G) sfor_o<3 * IC(q), 3>(gpre);
      });
      if constexpr (cb == X_G - 2) flag_load<255>(rws, wflag_vo, wflag_so);
      if constexpr (cb == X_T) {
        RBAR();
        v2u r0[8], r1[8];
        sfor<5>([&](auto i) { trr<2048 * IC(i)>(r0[IC(i)], ra[0]); trr<2048 * IC(i) + 1024>(r1[IC(i)], ra[0]); });
        trr<0>(r0[5], ra[1]); trr<2048>(r1[5], ra[1]); trr<1024>(r0[6], ra[1]); trr<3072>(r1[6], ra[1]); trr<0>(r0[7], ra[2]); trr<2048>(r1[7], ra[2]);
        tr_wait8(r0); tr_wait8(r1);
        for (int i = 0; i < 8; ++i) rt[i] = cat(r0[i], r1[i]);
      }
      if constexpr (cb == X_T + 1) { rcs = 0.f; for (int i = 0; i < 5; ++i) for (int e = 0; e < 4; ++e) rcs = dot2(rt[i][e], 0x3F803F80u, rcs); PIN(rcs); }
    });
    waitl(); sfor<4>([&](auto q) { xstat1(ic<9>{}, q); });
    mfma_done();
    for (int m = 0; m < 2; ++m) {
      const float s = pp == 0 ? S[m][0] : pp == 1 ? S[m][1] : pp == 2 ? S[m][2] : S[m][3];
      const float q = pp == 0 ? G[m][0] : pp == 1 ? G[m][1] : pp == 2 ? G[m][2] : G[m][3];
      const float mean = s * (1.f / 640.f), var = fmaxf(q * (1.f / 640.f) - mean * mean, 0.f), rs = __builtin_amdgcn_rsqf(var + 1e-5f);
      if (gm == q4) { lds_st<float>(sb1 + m * 64, rs); lds_st<float>(sb1 + 128 + m * 64, -mean); }
    }
    ST(29);
    waitv<7>();
    RBAR();
    ST(25);
    v4u wf[8];
    float cp[8];
    for (int i = 0; i < 7; ++i) wf[i] = lds_ld<v4u>(LA(i * 1024));
    auto rt_store = [&](auto i) {
      if constexpr (IC(i) < 5) st16(rt[IC(i)], rws, ro_[IC(i)], WS_WC + g * WC_G + ((5 * p + IC(i)) & 1) * 512);
      else if constexpr (IC(i) < 7) st16(rt[IC(i)], rws, fvo, wdk + (IC(i) - 5) * 256);
      else if constexpr (IC(i) == 7) st16(rt[7], rws, ro_[6], wdk);
      else st4(fu(rcs), rws, ro_[5], WS_CSC + (g * 16 + p) * 1024); };
    GAP();
    sfor<100>([&](auto n_) {
      constexpr int n = IC(n_), ks = n / 5, t = n % 5;
      if constexpr (n == 13) { waitv<AP.before[n / 2 + 1]>(); RBAR(); }
      if constexpr (n == 33) { ST(33); waitv<AP.before[n / 2 + 1] - AP.glast[2] - 1>(); RBAR(); ST(34); }
      if constexpr (n == 53) { ST(35); waitv<AP.before[n / 2 + 1] - AP.glast[3] - 1>(); RBAR(); ST(36); }
      if constexpr (n == 73) { ST(37); waitv<AP.before[n / 2 + 1] - AP.glast[4] - 1>(); RBAR(); ST(38); }
      if constexpr (n % 4 == 0) PIN(wf[(n + 3) & 7]);
      if constexpr (n + 7 < 100) wf[(n + 7) & 7] = lds_ld<v4u>(LA((n + 7) * 1024));
      m16_av<4 * ks, ks == 0>(kvacc[0][t], wf[n & 7]);
      if constexpr (n % 2 == 0) {
        constexpr int k = AP.kind[n / 2], i = AP.idx[n / 2];
        if constexpr (k == 1) gop(ic<i>{});
        if constexpr (k == 2) rt_store(ic<i>{});
        if constexpr (k == 3) { if (i < 10 || w < 2) dma_p<i % 5>(l16, rws, wa + 102400 + rblk(4 * (i / 5) + w) * 5120, L0 + 100 * K1 + (4 * (i / 5) + w) * 5120); }
        if constexpr (k == 4) woload(ic<i>{});
      }
      m16_av<4 * (20 + ks), ks == 0>(kvacc[1][t], wf[n & 7]);
      if constexpr (n >= 55 && n < 63 && n % 2 == 1) { cp[n - 55] = lds_ld<float>(csa_ + (n - 55) * 512); cp[n - 54] = lds_ld<float>(csa_ + (n - 54) * 512); }
      if constexpr (n == 67) lds_st<float>(csa_, ((cp[0] + cp[1]) + (cp[2] + cp[3])) + ((cp[4] + cp[5]) + (cp[6] + cp[7])));
      if constexpr (n >= 85 && n < 95 && n % 2 == 1) { ck[n - 85] = lds_ld<float>(ckb + ((n - 85) / 2) * 64); ck[n - 84] = lds_ld<float>(ckb + 4096 + ((n - 85) / 2) * 64); }
      if constexpr (n == 77 || n == 79) { constexpr int m = (n - 77) / 2; rsr[m] = lds_ld<f32x4>(sb4 + m * 64); nmr[m] = lds_ld<f32x4>(sb4 + 128 + m * 64); }
      if constexpr (n == 81 || n == 83) { constexpr int m = (n - 81) / 2; rsl[m] = lds_ld<float>(sb1 + m * 64); nml[m] = lds_ld<float>(sb1 + 128 + m * 64); }
      GAP();
    });
  }
  f32x2 csk[5];
  for (int t = 0; t < 5; ++t) csk[t] = bc2(ck[2 * t] + ck[2 * t + 1]);
  mfma_done();
  ST(4);
  {
    v4u T[5], T2b;
    sfor<5>([&](auto t_) {
      constexpr int t = IC(t_);
      u32 o[4];
      sfor<(N_E + 4 - t) / 5>([&](auto i) { woload(ic<AP.nwo + 5 * IC(i) + t>{}); });
      for (int m = 0; m < 2; ++m) for (int h = 0; h < 2; ++h) {
        const f32x2 a = h ? hi2(kvacc[m][t]) : lo2(kvacc[m][t]), nm = h ? hi2(nmr[m]) : lo2(nmr[m]), rs = h ? hi2(rsr[m]) : lo2(rsr[m]);
        const f32x2 d = fma2(nm, csk[t], a);
        if (t < 3) o[2 * m + h] = cvt2(phi2(fma2(d, rs, bc2(1e-6f))));
        if (t > 2) o[2 * m + h] = cvt2(d * rs);
        if (t == 2) T2b[2 * m + h] = c < 8 ? 0x3F803F80u : cvt2(d * rs);
      }
      T[t] = v4u{o[0], o[1], o[2], o[3]};
      PIN(T[t]);
    });
    f32x4 R[7];
    m16_vv<true>(R[0], T[0], T[3]); m16_vv<true>(R[1], T[1], T[4]); m16_vv<true>(R[2], T[0], T2b); m16_vv<true>(R[3], T[1], T2b);
    m16_vv<true>(R[4], T[2], T[3]); m16_vv<true>(R[5], T[2], T[4]); m16_vv<true>(R[6], T[2], T2b);
    GAP(); asm volatile("s_nop 7" ::: "memory"); GAP();
    for (int i = 0; i < 7; ++i) lds_st<f32x4>(lb0 + KVP_L + w * 7168 + i * 1024, R[i]);
  }
  waitv<N_E>();
  BAR();
  ST(5);
  v4u pq0[2], pq1[2], part[16];
  const u32 wdb = WS_WD + n80 * 286720 + w * 71680;
  const u32 wcb = WS_WC + g * WC_G;
  v4u wdv[NV > 0 ? NV : 1], xr0, xr1; v2u xr2;
  auto pre = [&](auto o) {
    constexpr int i = IC(o);
    if constexpr (i < 4) dma_p<i>(l16, rws, WS_CSC + g * 16384 + w * 4096, L0 + 84 * K1 + w * 4096);
    else if constexpr (i < 6) woload(ic<(i < 6 ? 19 + i : 0)>{});
    else if constexpr (i < 27) dma_p<(i - 6) % 7>(l16, rws, wcb + (4 * ((i - 6) / 7) + w) * 7168, L0 + (4 * ((i - 6) / 7) + w) * 7168);
    else if constexpr (i < 51) ld_a<60 + 4 * (i - 27), ((i - 25) & 3) * 1024>(l16, rws, wdb + ((i + 19) >> 2) * 4096);
    else if constexpr (i < 51 + NV) ld_v(wdv[i < 51 + NV ? i - 51 : 0], l16, rws, wdb + (46 - NV + i - 51) * 1024);
  };
  {
    f32x4 qa[2][3];
    v4u wf[8];
    auto qaddr = [&](int n) -> u32 { return (n % 3 < 2 ? qb1 + (n % 3) * 1024 : qb2) + (n / 3) * 2560; };
    {
      const f32x4 s0 = lds_ld<f32x4>(kva), s1 = lds_ld<f32x4>(kva + 7168), s2 = lds_ld<f32x4>(kva + 2 * 7168), s3 = lds_ld<f32x4>(kva + 3 * 7168);
      for (int i = 0; i < 7; ++i) wf[i] = lds_ld<v4u>(qaddr(i));
      st16(__builtin_bit_cast(v4u, (s0 + s1) + (s2 + s3)), rws, kvo, WS_KVP + (g * 16 + p) * 3456);
    }
    ST(6);
    u32 oa[8];
    auto kvload = [&](auto i) { ld_v(part[IC(i)], kvo, rws, WS_KVP + (g * 16 + IC(i)) * 3456); };
    auto hop1 = [&]() {
      {
        const u32 tb = L0 + KVS, zr = tb + 1680;
        const u32 ea = pp == 0 ? 16 + q4 : 20, eb = pp == 1 ? 16 + q4 : 20;
        const bool va = (c & 1) == 0, vb = (c & 1) == 1;
        oa[0] = tb + c * 40 + gm * 8;                      oa[1] = gm == 0 ? tb + c * 40 + 32 : zr;
        oa[2] = tb + (21 + c) * 40 + gm * 8;               oa[3] = gm == 1 ? tb + (21 + c) * 40 + 32 : zr;
        oa[4] = va ? tb + ea * 40 + gm * 8 : zr;           oa[5] = (va && gm == 0) ? tb + ea * 40 + 32 : zr;
        oa[6] = vb ? tb + (21 + eb) * 40 + gm * 8 : zr;    oa[7] = (vb && gm == 1) ? tb + (21 + eb) * 40 + 32 : zr;
        for (int i = 0; i < 8; ++i) PIN(oa[i]);
      }
      ST(7); waitv<0>();
      xr0 = lds_ld<v4u>(xra + gm * 8); xr1 = lds_ld<v4u>(xra + gm * 8 + 64); xr2 = lds_ld<v2u>(xra + 128);
      flag_check<254>(rws, wflag_vo, wflag_so + 32768, tag); ST(8); };
    constexpr int QW = 23 - AP.nwo - N_E;
    static_assert(2 * QW + 8 < 3 * (Q_KV - 2), "Q loop slots");
    constexpr int XR0 = QW > 3 * Q_F1 / 2 + 1 ? QW : 3 * Q_F1 / 2 + 1;
    sfor<60>([&](auto n_) {
      constexpr int n = IC(n_), ks = n / 3, t = n % 3;
      if constexpr (n == 3 * Q_F1) { waitv<(3 * Q_F1 / 2 < QW ? 3 * Q_F1 / 2 : QW)>(); RBAR(); if (w == 0 && lane == 0) st4(tag, rws, 0, myflag + 32768); }
      if constexpr (n == 3 * (Q_KV - 2)) flag_load<254>(rws, wflag_vo, wflag_so + 32768);
      if constexpr (n == 3 * Q_KV) hop1();
      if constexpr (n % 4 == 0) PIN(wf[(n + 3) & 7]);
      if constexpr (n + 7 < 60) wf[(n + 7) & 7] = lds_ld<v4u>(qaddr(n + 7));
      m16_va<4 * ks, ks == 0>(qa[0][t], wf[n & 7]);
      if constexpr (n % 2 == 1 && n / 2 < QW) woload(ic<AP.nwo + N_E + (n / 2 < QW ? n / 2 : 0)>{});
      if constexpr (n % 2 == 1 && n / 2 >= XR0 && n / 2 < XR0 + 3)
        dma<1, 2>(xrv, rx, (128 * p + 64 * half + 16 * w + 6 * (n / 2 - XR0)) * 1280 + n80 * 160, L0 + (8 + w) * 7168 + (n / 2 - XR0) * K1);
      if constexpr (n >= 3 * Q_KV && n - 3 * Q_KV < 16) kvload(ic<(n >= 3 * Q_KV && n - 3 * Q_KV < 16 ? n - 3 * Q_KV : 0)>{});
      m16_va<4 * (20 + ks), ks == 0>(qa[1][t], wf[n & 7]);
      GAP();
    });
    if constexpr (Q_KV == 20) hop1();
    sfor<16>([&](auto i) { if constexpr (IC(i) >= 3 * (20 - Q_KV)) kvload(i); });
    PIN(xr0); PIN(xr1); PIN(xr2);
    sfor<P_C>(pre);
    f32x4 cs[3];
    for (int t = 0; t < 3; ++t) cs[t] = lds_ld<f32x4>(L0 + 152 * K1 + ((5 + t) * 16 + 4 * gm) * 4) + lds_ld<f32x4>(L0 + 156 * K1 + ((5 + t) * 16 + 4 * gm) * 4);
    mfma_done();
    for (int m = 0; m < 2; ++m) {
      u32 pk[3][2];
      for (int t = 0; t < 3; ++t) {
        pk[t][0] = cvt2(phi2(fma2(fma2(bc2(nml[m]), lo2(cs[t]), lo2(qa[m][t])), bc2(rsl[m]), bc2(1e-6f))));
        pk[t][1] = cvt2(phi2(fma2(fma2(bc2(nml[m]), hi2(cs[t]), hi2(qa[m][t])), bc2(rsl[m]), bc2(1e-6f))));
      }
      pq0[m] = v4u{pk[0][0], pk[0][1], pk[2][0], pk[2][1]};
      pq1[m] = v4u{pk[1][0], pk[1][1], pk[2][0], pk[2][1]};
      PIN(pq0[m]); PIN(pq1[m]);
    }
    waitv<P_C>();
    asm volatile("; LANDED" : "+v"(part[0]), "+v"(part[1]), "+v"(part[2]), "+v"(part[3]), "+v"(part[4]), "+v"(part[5]), "+v"(part[6]), "+v"(part[7]),
                 "+v"(part[8]), "+v"(part[9]), "+v"(part[10]), "+v"(part[11]), "+v"(part[12]), "+v"(part[13]), "+v"(part[14]), "+v"(part[15])::"memory");
    f32x4 s = __builtin_bit_cast(f32x4, part[0]);
    for (int i = 1; i < 16; ++i) s += __builtin_bit_cast(f32x4, part[i]);
    lds_st<v2u>(L0 + KVS + threadIdx.x * 8, v2u{cvt2(lo2(s)), cvt2(hi2(s))});
    BAR();
    ST(9);
    v4u A[4];
    for (int i = 0; i < 4; ++i) A[i] = cat(lds_ld<v2u>(oa[2 * i]), lds_ld<v2u>(oa[2 * i + 1]));
    f32x4 O0[2], O1[2], O2[2];
    for (int m = 0; m < 2; ++m) { m16_vv<true>(O2[m], A[2], pq0[m]); m16_vv<false>(O2[m], A[3], pq1[m]); }
    for (int m = 0; m < 2; ++m) { m16_vv<true>(O0[m], A[0], pq0[m]); m16_vv<true>(O1[m], A[1], pq1[m]); }
    GAP(); asm volatile("s_nop 7" ::: "memory"); GAP();
    for (int m = 0; m < 2; ++m) {
      const f32x2 dn = hi2(O2[m]) + 1e-6f, r = {__builtin_amdgcn_rcpf(dn[0]), __builtin_amdgcn_rcpf(dn[1])};
      const u32 so = WS_O + (8 * p + 2 * w + m) * 20480;
      st16(v4u{cvt2(lo2(O0[m]) * r[0]), cvt2(hi2(O0[m]) * r[0]), cvt2(lo2(O1[m]) * r[1]), cvt2(hi2(O1[m]) * r[1])}, rws, l16, so + g * 1024);
      st4(cvt2(lo2(O2[m]) * r), rws, c * 16 + gm * 4, so + 16384 + g * 256);
    }
    ST(22);
    sfor_o<P_C, P_D - P_C>(pre);
    waitv<P_D - P_C>(); BAR();
  }
  ST(10);
  if (w == 0 && lane == 0) st4(tag, rws, 0, myflag + 2 * 32768);
  u32 pvo = (c & 3) * 128;
  sfor_o<P_D, P_E - P_D>(pre);
  flag_load<59>(rws, pvo, WS_FLG + 2 * 32768 + p * 2048 + w * 512);
  sfor_o<P_E, P_F - P_E>(pre);
  f32x4 bacc[4][5];
  u32 sw[3], sr = L0 + 100 * K1 + l16 + w * 15 * K1, hv0 = gm * 512 + c * 16, hv1 = (gm >> 1) * 512 + c * 16 + (gm & 1) * 8, hv2 = gm == 0 ? c * 8 : OOB, cspk;
  {
    for (int j = 0; j < 3; ++j) { sw[j] = L0 + 100 * K1 + l16 + (((w + 1 + j) & 3) * 3 + j) * 5 * K1; PIN(sw[j]); }
    PIN(sr); PIN(hv0); PIN(hv1); PIN(hv2); PIN(pvo);
    const u32 xr[10] = {xr0.x, xr0.y, xr0.z, xr0.w, xr1.x, xr1.y, xr1.z, xr1.w, xr2.x, xr2.y};
    for (int t = 0; t < 5; ++t) { bacc[3][t] = f32x4{bflo(xr[2 * t]), bfhi(xr[2 * t]), bflo(xr[2 * t + 1]), bfhi(xr[2 * t + 1])}; PIN(bacc[3][t]); }
  }
  ST(11);
  waitv<P_F - P_E>();
  flag_check<59>(rws, pvo, WS_FLG + 2 * 32768 + p * 2048 + w * 512, tag);
  ST(12);
  u32 hp[10];
  {
    auto oload = [&](auto i) {
      constexpr int j = IC(i) / 5, k = IC(i) % 5;
      const u32 so = WS_O + (8 * p + 4 * half + ((w + 1 + j) & 3)) * 20480;
      if constexpr (k < 4) ld_a<4 * (IC(i) % 15), k * 1024>(l16, rws, so + w * 4096);
      else ld_a<4 * (IC(i) % 15), 0>(l16, rws, so + (16 + w) * 1024);
    };
    constexpr int UP = 9;
    sfor<UP>(oload);
    {
      static_assert(P_C >= 4, "the partials are older than the o stores");
      u32 ca = L0 + 84 * K1 + threadIdx.x * 4;
      PIN(ca);
      float cv[16];
      for (int i = 0; i < 16; ++i) cv[i] = lds_ld<float>(ca + i * 1024);
      float csv = 0.f;
      for (int i = 0; i < 16; ++i) csv += cv[i];
      const u32 hl = cvtpk(csv, 0.f) & 0xffffu;
      cspk = hl | (cvtpk(csv - bflo(hl), 0.f) << 16);
      PIN(cspk);
    }
    constexpr auto bcum = [](int i) { const int k = 2 * (UP + i - 20); return k < 0 ? 0 : k > P_G - P_F ? P_G - P_F : k; };
    sfor<20>([&](auto i) {
      constexpr int j = IC(i) / 5, k = IC(i) % 5, issued = (UP + IC(i) < 20 ? UP + IC(i) : 20 + bcum(IC(i)));
      waitv<issued - IC(i) - 1>();
      sfor<5>([&](auto t) {
        constexpr int f = 5 * k + IC(t), tt = 2 * (k - 2) + IC(t) - 3;
        m16_aa<wo_reg(f), 4 * (IC(i) % 15), (k == 0 && j < 3)>(bacc[j][IC(t)]);
        if constexpr (IC(t) == 1 && UP + IC(i) < 20) oload(ic<(UP + IC(i) < 20 ? UP + IC(i) : 0)>{});
        if constexpr ((IC(t) == 0 || IC(t) == 2) && bcum(IC(i)) + IC(t) / 2 < bcum(IC(i) + 1)) pre(ic<P_F + bcum(IC(i)) + IC(t) / 2>{});
        if constexpr (j > 0 && k >= 2 && IC(t) >= 3 && tt < 5) { GAP(); lds_st<f32x4>(sw[j > 0 ? j - 1 : 0] + (tt < 5 ? tt : 0) * K1, bacc[j > 0 ? j - 1 : 0][tt < 5 ? tt : 0]); GAP(); }
      });
    });
    ST(13);
    BAR();
    f32x4 pr[3][5];
    for (int j = 0; j < 3; ++j) for (int t = 0; t < 5; ++t) pr[j][t] = lds_ld<f32x4>(sr + (j * 5 + t) * K1);
    GAP();
    for (int t = 0; t < 5; ++t) {
      const f32x4 s = (bacc[3][t] + pr[0][t]) + (pr[1][t] + pr[2][t]);
      hp[2 * t] = cvt2(lo2(s)); hp[2 * t + 1] = cvt2(hi2(s));
    }
    const u32 so = WS_H + (4 * p + 2 * half + (w >> 1)) * 40960 + 5 * n80 * 1024 + (w & 1) * 256;
    st16(v4u{hp[0], hp[1], hp[2], hp[3]}, rws, hv0, so);
    st16(v4u{hp[4], hp[5], hp[6], hp[7]}, rws, hv0, so + 2048);
    st8(v2u{hp[8], hp[9]}, rws, hv1, so + 4096);
    asm volatile("" ::: "memory");
    float s1 = 0.f, s2 = 0.f;
    for (int i = 0; i < 10; ++i) { s1 = dot2(hp[i], 0x3F803F80u, s1); s2 = dot2(hp[i], hp[i], s2); }
    {
      u32 a = fu(s1), a2 = a, d = fu(s2), d2 = d;
      PIN(a2); PIN(d2);
      auto r1 = __builtin_amdgcn_permlane16_swap(a, a2, false, false);
      auto r2 = __builtin_amdgcn_permlane16_swap(d, d2, false, false);
      a = fu(uf(r1[0]) + uf(r1[1])); d = fu(uf(r2[0]) + uf(r2[1])); a2 = a; d2 = d;
      PIN(a2); PIN(d2);
      auto r3 = __builtin_amdgcn_permlane32_swap(a, a2, false, false);
      auto r4 = __builtin_amdgcn_permlane32_swap(d, d2, false, false);
      s1 = uf(r3[0]) + uf(r3[1]); s2 = uf(r4[0]) + uf(r4[1]);
    }
    st8(v2u{fu(s1), fu(s2)}, rws, hv2, WS_HST + (p * 8 + n80) * 1024 + (64 * half + 16 * w) * 8);
  }
  ST(23);
  sfor_o<P_G, P_H - P_G>(pre);
  waitv<P_H - P_G>(); BAR();
  ST(14);
  if (w == 0 && lane == 0) {
    st4(tag, rws, 0, myflag + 3 * 32768);
    if (b == 0) st4(tag, rws, 0, WS_EPOCH);
  }
  static_assert(P_H >= 27 && P_G - P_F <= 18, "ring groups 0..2 are out before groups 3, 4");
  auto pre3 = [&](auto o) {
    constexpr int i = IC(o);
    if constexpr (i < 14) dma_p<i % 7>(l16, rws, wcb + (12 + 4 * (i / 7) + w) * 7168, L0 + (12 + 4 * (i / 7) + w) * 7168);
    else pre(ic<P_H + i - 14>{}); };
  constexpr int N3 = 14 + 51 + NV - P_H;
  u32 p3v = ((c & 3) + 8 * ((c >> 2) & 1)) * 128;
  sfor<N3 - P_T>(pre3);
  flag_load<59>(rws, p3v, WS_FLG + 3 * 32768 + p * 2048 + (w >> 1) * 512);
  sfor_o<N3 - P_T, P_T>(pre3);
  lds_st<v4u>(L0 + 140 * K1 + threadIdx.x * 16, v4u{cspk, cspk, 0u, 0u});
  u32 ab[4], afl;
  const u32 aflag = WS_FLG + p * 2048 + w * 512;
  auto aload = [&](auto i) {
    constexpr int k = d_kap(IC(i) < 40 ? IC(i) / 4 : 10 + (IC(i) - 40) % 4), j = IC(i) < 40 ? IC(i) % 4 : (IC(i) - 40) / 4;
    ld_a<act_reg(IC(i)), (k & 3) * 1024>(l16, rws, ab[j] + (k >> 2) * 4096); };
  u32 wb0 = lb0 + w * 7168, wb1 = wb0 + 57344, rd_[3], oo = (128 * p + 64 * half + 16 * w + c) * 1280 + n80 * 160 + gm * 16;
  for (int j = 0; j < 3; ++j) { rd_[j] = lb0 + (4 * j + ((w + 3 - j) & 3)) * 7168; PIN(rd_[j]); }
  PIN(wb0); PIN(wb1); PIN(oo);
  {
    const u32 tok = lane & 31, hh = lane >> 5;
    u32 avo = (tok >> 4) * 57344 + hh * 256 + (tok & 15) * 16, csa = L0 + 140 * K1 + tok * 16, hsa = L0 + 144 * K1 + w * 2048 + tok * 8;
    PIN(avo); PIN(csa); PIN(hsa);
    f32x16 acc[7];
    v4u wf[8], csf[3], mfr, apk;
    for (int i = 0; i < 7; ++i) wf[i] = lds_ld<v4u>(LA(i * 1024));
    ST(31);
    waitv<P_T>();
    flag_check<59>(rws, p3v, WS_FLG + 3 * 32768 + p * 2048 + (w >> 1) * 512, tag);
    ST(15);
    auto hload = [&](auto i) { ld_a<h_reg(IC(i)), (IC(i) & 3) * 1024>(l16, rws, WS_H + (4 * p + w) * 40960 + (IC(i) >> 2) * 4096); };
    auto refill = [&](auto i) {
      constexpr int G = CG0 + IC(i) / 7, q = IC(i) % 7;
      if constexpr (G < 10) dma_p<q>(l16, rws, wcb + (4 * G + w) * 7168, L0 + (4 * (G % 5) + w) * 7168);
      else dma_p<q>(l16, rws, wdb + (G - 10) * 7168, L0 + (4 * (G - 10) + w) * 7168);
    };
    sfor<2>([&](auto i) { dma<1, 1>(fvo, rws, WS_HST + (p * 8 + 4 * IC(i)) * 1024 + w * 256, L0 + 144 * K1 + w * 2048 + IC(i) * 1024); });
    sfor<C_UP>(hload);
    const u32 aso = WS_ACT + (8 * p + 2 * w) * 57344 + 7 * g * 512;
    auto tso = [&](u32 u) -> u32 { return aso + ((g & 1) ? 6 - u : u) * 512; };
    f32x2 ssum;
    float var, ke, ee, pr_, dd, av[2];
    GAP();
    auto cstep = [&](auto f_) {
      constexpr int f = IC(f_), ks = f < 120 ? f / 3 : f < 200 ? (f - 120) / 2 : f % 40, u = f < 120 ? f % 3 : f < 200 ? 3 + f % 2 : f / 40;
      if constexpr (f > 0 && f % 28 == 0) {
        constexpr int j = f / 28 - 1;
        if constexpr (j + 2 >= CG0 && j + 2 < 10) waitv<CP.before[f] - CP.rlast[j + 2] - 1>();
        if constexpr (f == 196) waitv<CP.before[196] - CP.before[C_E0 + 2 * 26]>();
        if constexpr (f == 252) waitv<CP.before[252] - CP.before[C_E0 + 4 * 26]>();
        RBAR();
        if constexpr (f == 196 || f == 252) { if (w == 0 && lane == 0) st4(tag, rws, 0, myflag + (f == 196 ? 5 : 6) * 32768); }
      }
      if constexpr (f < 120 && f % 3 == 0) waitv<CP.before[f] - CP.hpos[ks] - 1>();
      if constexpr (f % 4 == 0) PIN(wf[(f + 3) & 7]);
      if constexpr (f + 7 < 280) wf[(f + 7) & 7] = lds_ld<v4u>(LA(((f + 7) % 140) * 1024));
      m32_va<h_reg(ks), ks == 0>(acc[u], wf[f & 7]);
      if constexpr (CP.kind[f] == 1) hload(ic<CP.idx[f]>{});
      if constexpr (CP.kind[f] == 2) refill(ic<CP.idx[f]>{});
      if constexpr (f == 1) { asm volatile("; LANDED" ::: "memory"); for (int i = 0; i < NV; ++i) PIN(wdv[i]); }
      if constexpr (CP.kind[f] == 5) aload(ic<CP.idx[f]>{});
      if constexpr (CP.kind[f] == 6) flag_ldv(afl, rws, pvo, aflag + (5 + CP.idx[f]) * 32768);
      if constexpr (f == C_CK) { waitv<CP.before[f + 1] - CP.fpos[0] - 1>(); flag_ckv(afl, rws, pvo, aflag + 5 * 32768, tag); }
      if constexpr (f == 100) for (int j = 0; j < 4; ++j) { ab[j] = WS_ACT + (8 * p + 4 * half + ((w + 1 + j) & 3)) * 57344 + w * 14336; asm volatile("" : "+s"(ab[j])); }
      if constexpr (f == 40) ssum = lds_ld<f32x2>(hsa);
      if constexpr (f > 40 && f <= 68 && f % 4 == 0) { ssum += lds_ld<f32x2>(hsa + ((f - 40) / 4) * 256); PIN(ssum); }
      if constexpr (f == 76) {
        const float mean = ssum[0] * (1.f / 640.f);
        var = fmaxf(ssum[1] * (1.f / 640.f) - mean * mean, 0.f) + 1e-5f;
        ke = __builtin_amdgcn_rsqf(var) * -LOG2E;
        const float nm = hh ? 0.f : -mean;
        const u32 h2 = cvtpk(nm, nm), l2 = cvtpk(nm - bflo(h2), nm - bflo(h2));
        mfr = v4u{h2, l2, 0u, 0u};
        PIN(var); PIN(ke); PIN(mfr);
      }
      if constexpr (f == 108 || f == 110 || f == 112) csf[(f - 108) / 2] = lds_ld<v4u>(csa + ((f - 108) / 2) * 512);
      if constexpr (f == 190 || f == 192) csf[(f - 190) / 2] = lds_ld<v4u>(csa + (3 + (f - 190) / 2) * 512);
      if constexpr (f == 232 || f == 272) csf[0] = lds_ld<v4u>(csa + (f / 40) * 512);
      if constexpr (f == 119) { GAP(); for (int i = 0; i < 3; ++i) m32_vv(acc[i], csf[i], mfr); ST(30); }
      if constexpr (f == 199) { GAP(); for (int i = 0; i < 2; ++i) m32_vv(acc[3 + i], csf[i], mfr); }
      if constexpr (f == 239 || f == 279) { GAP(); m32_vv(acc[f / 40], csf[0], mfr); }
      if constexpr (f >= C_E0) {
        constexpr int e = (f - C_E0) / 26, s = (f - C_E0) % 26, r = s / 3;
        if constexpr (s < 24 && s % 3 == 0) {
          if constexpr (r > 0) asm volatile("v_mul_f32 %0, %1, %2" : "=v"(av[(r - 1) & 1]) : "v"(pr_), "v"(dd));
          asm volatile("v_mul_f32 %0, %2, %3\n\tv_mul_f32 %1, %2, %4\n\tv_exp_f32 %0, %0" : "=&v"(ee), "=&v"(pr_) : "v"(acc[e][r]), "v"(ke), "v"(acc[e][8 + r]));
        }
        if constexpr (s < 24 && s % 3 == 1) {
          asm volatile("v_fma_f32 %0, %1, %2, %2\n\tv_rcp_f32 %0, %0" : "=&v"(dd) : "v"(ee), "v"(var));
          if constexpr (r >= 2 && r % 2 == 0) { u32 t; asm volatile("v_cvt_pk_bf16_f32 %0, %1, %2" : "=v"(t) : "v"(av[0]), "v"(av[1])); apk[r / 2 - 1] = t; }
        }
        if constexpr (s == 24) { u32 t; asm volatile("v_mul_f32 %1, %2, %3\n\tv_cvt_pk_bf16_f32 %0, %4, %1" : "=v"(t), "=&v"(av[1]) : "v"(pr_), "v"(dd), "v"(av[0])); apk[3] = t; }
        if constexpr (s == 25) st16(apk, rws, avo, tso(e));
      }
      GAP();
    };
    sfor_o<0, 160>(cstep); sfor_o<160, 120>(cstep);
    mfma_done();
    ST(16);
    {
      u32 o[4];
      for (int i = 0; i < 4; ++i) {
        const f32x2 gv = {acc[6][2 * i], acc[6][2 * i + 1]}, uv = {acc[6][8 + 2 * i], acc[6][9 + 2 * i]}, t = gv * ke;
        const f32x2 d = fma2(f32x2{__builtin_amdgcn_exp2f(t[0]), __builtin_amdgcn_exp2f(t[1])}, bc2(var), bc2(var));
        o[i] = cvt2((gv * uv) * f32x2{__builtin_amdgcn_rcpf(d[0]), __builtin_amdgcn_rcpf(d[1])});
      }
      st16(v4u{o[0], o[1], o[2], o[3]}, rws, avo, tso(6));
    }
    ST(17);
  }
  {
    f32x4 acc[4][5];
    auto dop = [&](auto o) {
      constexpr int k = DP.kind[IC(o)], i = DP.idx[IC(o)];
      if constexpr (k == 1) aload(ic<i>{});
      if constexpr (k == 4) flag_ldv(afl, rws, pvo, aflag + 4 * 32768);
    };
    v4u wf[2][5];
    auto wl = [&](int f) -> u32 { return (f < 14 ? wb0 : wb1) + ((f / 7) & 1) * 28672 + (f % 7) * 1024; };
    for (int t = 0; t < 5; ++t) wf[0][t] = lds_ld<v4u>(wl(t));
    for (int t = 0; t < 5; ++t) { acc[3][t] = f32x4{bflo(hp[2 * t]), bfhi(hp[2 * t]), bflo(hp[2 * t + 1]), bfhi(hp[2 * t + 1])}; PIN(acc[3][t]); }
    sfor<D_UP>(dop);
    GAP();
    sfor<14>([&](auto k) {
      constexpr int st = IC(k);
      waitv<DP.before[6 * st] - DP.need[st] - 1>();
      if constexpr (st == D_CK1) { flag_ckv(afl, rws, pvo, aflag + 6 * 32768, tag); ST(19); }
      if constexpr (st == D_PUB) { RBAR(); ST(18); if (w == 0 && lane == 0) st4(tag, rws, 0, myflag + 4 * 32768); }
      if constexpr (st == D_CHK) flag_ckv(afl, rws, pvo, aflag + 4 * 32768, tag);
      auto slot = [&](auto m) {
        if constexpr (IC(m) % 3 == 1 && IC(m) < 18) { constexpr int sl = 6 * st + IC(m) / 3; if constexpr (DP.before[sl + 1] > DP.before[sl]) dop(ic<DP.before[sl]>{}); } };
      if constexpr (st < 10) {
        sfor<5>([&](auto t) {
          if constexpr (st < 6 && 5 * (st + 1) + IC(t) < 46 - NV) wf[(st + 1) & 1][IC(t)] = lds_ld<v4u>(wl(5 * (st + 1) + IC(t)));
          sfor<4>([&](auto j) {
            constexpr int f = 5 * st + IC(t);
            if constexpr (f >= 46 - NV && f < 46) m16_va<act_reg(4 * st + IC(j)), false>(acc[IC(j)][IC(t)], wdv[f >= 46 - NV && f < 46 ? f - 46 + NV : 0]);
            else if constexpr (st < 7) m16_va<act_reg(4 * st + IC(j)), (st == 0 && IC(j) < 3)>(acc[IC(j)][IC(t)], wf[st & 1][IC(t)]);
            else m16_aa<wd_reg(5 * st + IC(t)), act_reg(4 * st + IC(j)), false>(acc[IC(j)][IC(t)]);
            slot(ic<4 * IC(t) + IC(j)>{});
          });
          GAP();
        });
      } else {
        constexpr int j = st - 10;
        sfor<4>([&](auto q) {
          sfor<5>([&](auto t) {
            m16_aa<wd_reg(5 * (10 + IC(q)) + IC(t)), act_reg(4 * st + IC(q)), false>(acc[j][IC(t)]);
            slot(ic<5 * IC(q) + IC(t)>{});
            constexpr int tt = 5 * IC(q) + IC(t) - 10;
            if constexpr (j > 0 && tt >= 0 && tt % 2 == 0) { GAP(); lds_st<f32x4>((j == 3 ? wb1 : wb0) + (j == 2 ? 28672 : 0) + (tt / 2) * 1024, acc[j > 0 ? j - 1 : 0][tt >= 0 ? tt / 2 : 0]); GAP(); }
          });
        });
      }
    });
    ST(20);
    BAR();
    f32x4 pr[3][5];
    for (int j = 0; j < 3; ++j) for (int t = 0; t < 5; ++t) pr[j][t] = lds_ld<f32x4>(rd_[j] + t * 1024);
    GAP();
    u32 op[10];
    for (int t = 0; t < 5; ++t) {
      const f32x4 s = (acc[3][t] + pr[0][t]) + (pr[1][t] + pr[2][t]);
      op[2 * t] = cvt2(lo2(s)); op[2 * t + 1] = cvt2(hi2(s));
    }
    const rsrc_t ro = mkrs(out_, 2048 * 640 * 2);
    __builtin_amdgcn_raw_buffer_store_b128(v4u{op[0], op[1], op[2], op[3]}, ro, oo, 0, 0);
    __builtin_amdgcn_raw_buffer_store_b128(v4u{op[4], op[5], op[6], op[7]}, ro, oo + 64, 0, 0);
    __builtin_amdgcn_raw_buffer_store_b64(v2u{op[8], op[9]}, ro, oo + 128 - gm * 8, 0, 0);
  }
  asm volatile(".Lfb_end:");
}

extern "C" void kernel_launch(void* const* d_in, const int* in_sizes, int n_in, void* d_out, int out_size, void* d_ws, size_t ws_size,
                              hipStream_t stream) {
  (void)hipFuncSetAttribute((const void*)fused_block, hipFuncAttributeMaxDynamicSharedMemorySize, LDS_BYTES);
  fused_block<<<256, 256, LDS_BYTES, stream>>>(d_in[0], d_in[1], d_in[2], d_in[4], d_in[5], d_in[6], d_out, d_ws);
}
```
